```python
import torch

def _softmax(x, axis=-1):
    return torch.softmax(x, dim=axis)
def _gelu(x):
    return torch.nn.functional.gelu(x, approximate="tanh")
def _silu(x):
    return torch.nn.functional.silu(x)
def _sigmoid(x):
    return torch.sigmoid(x)
def _layernorm(x, axis=-1, eps=1e-5):
    m = x.mean(dim=axis, keepdim=True)
    v = x.var(dim=axis, keepdim=True, unbiased=False)
    return (x - m) / torch.sqrt(v + eps)
def _scatter_add(tgt, idx, src, axis=0):
    return tgt.clone().index_add_(axis, idx, src)
def _segment_sum(src, seg, n):
    out = torch.zeros((n,) + src.shape[1:], dtype=src.dtype)
    return out.index_add_(0, seg, src)
def _pad(x, pw):
    flat = tuple(v for pair in reversed(pw) for v in pair)
    return torch.nn.functional.pad(x, flat)

kernel_name = 'standalone__cross_network_dcn__recsys'

def setup_inputs():

    _w = (lambda a: (lambda t: t.to(torch.bfloat16) if t.is_floating_point() else t)(torch.as_tensor(a)))
    B, F, D, L = 16384, 100, 16, 3
    Din = min(F * D, 1024)
    return {
        "x0": _w((lambda *s: torch.randn(*s, dtype=torch.bfloat16) * 0.05)(B, Din)),
        "ws": _w((lambda *s: torch.randn(*s, dtype=torch.bfloat16) * 0.8)(L, Din, Din) * (Din ** -0.5)),
        "bs": _w((lambda *s: torch.randn(*s, dtype=torch.bfloat16) * 3.2)(L, Din) * 0.01),
    }

def reference(x0, ws, bs):
    x = x0
    L = ws.shape[0]
    for l in range(L):
        proj = torch.einsum('bd,de->be', x, ws[l]) + bs[l]
        x = x0 * proj + x
    return x
```

```cpp
#include <hip/hip_runtime.h>
#include <hip/hip_bf16.h>
#include <stdint.h>

typedef unsigned u32x4 __attribute__((ext_vector_type(4)));
typedef float f32x4 __attribute__((ext_vector_type(4)));
typedef float f32x2 __attribute__((ext_vector_type(2)));
typedef short bf16x8 __attribute__((ext_vector_type(8)));
typedef __bf16 bf16x2 __attribute__((ext_vector_type(2)));
#define GAS __attribute__((address_space(1)))
#define LAS __attribute__((address_space(3)))

constexpr int D = 1024, NL = 3, NB = 16384;

__global__ __launch_bounds__(256) void retile(const unsigned short* __restrict__ W, const unsigned short* __restrict__ bs,
                                              u32x4* __restrict__ Wt, float* __restrict__ ctab) {
  const int tid = blockIdx.x * 256 + threadIdx.x;
  const int lane = tid & 63, wv = tid >> 6;
  const int a = lane & 3, g4 = (lane >> 2) & 3, q = lane >> 4;
  const int gq = wv & 7, s = (wv >> 3) & 31, l = wv >> 8;
  const int g = gq * 4 + g4;
  const unsigned short* src = W + (size_t)l * D * D + (size_t)(32 * s + 8 * q) * D + 32 * g + 8 * a;
  u32x4 r[8];
#pragma unroll
  for (int j = 0; j < 8; ++j) r[j] = *(const u32x4*)(src + (size_t)j * D);
  const int w = g >> 3, gl = g & 7;
#pragma unroll
  for (int c = 0; c < 8; ++c) {
    u32x4 o;
#pragma unroll
    for (int p = 0; p < 4; ++p) {
      const unsigned lo = r[2 * p][c >> 1], hi = r[2 * p + 1][c >> 1];
      o[p] = (c & 1) ? ((lo >> 16) | (hi & 0xffff0000u)) : ((lo & 0xffffu) | (hi << 16));
    }
    const int t = gl * 2 + (c >> 2), i = 4 * a + (c & 3);
    Wt[((((size_t)l * 4 + w) * 32 + s) * 16 + t) * 64 + i + 16 * q] = o;
  }
  if (tid < D) {
    float c = 1.0f;
#pragma unroll
    for (int ll = 0; ll < NL; ++ll) {
      c += __uint_as_float((unsigned)bs[ll * D + tid] << 16);
      ctab[ll * D + tid] = c;
    }
  }
}

__device__ __forceinline__ unsigned cvtpk(float lo, float hi) {
  f32x2 v = {lo, hi};
  bf16x2 b = __builtin_convertvector(v, bf16x2);
  return __builtin_bit_cast(unsigned, b);
}

__global__ __launch_bounds__(256, 1) void dcn_main(const unsigned short* __restrict__ x0, const u32x4* __restrict__ Wt,
                                                   const float* __restrict__ ctab, unsigned short* __restrict__ out) {
  extern __shared__ __attribute__((aligned(16))) unsigned char smem[];
  LAS u32x4* xs = (LAS u32x4*)smem;
  const int lane = threadIdx.x & 63, w = __builtin_amdgcn_readfirstlane(threadIdx.x >> 6);
  const int fr = lane & 15, fq = lane >> 4;
  const size_t row0 = (size_t)blockIdx.x * 64;
  u32x4 x0r[8][4];
#pragma unroll
  for (int g = 0; g < 8; ++g)
#pragma unroll
    for (int mt = 0; mt < 4; ++mt)
      x0r[g][mt] = *(const GAS u32x4*)(x0 + (row0 + mt * 16 + fr) * D + (8 * w + g) * 32 + fq * 8);
  const GAS u32x4* wp = (const GAS u32x4*)Wt + (size_t)w * 32 * 16 * 64 + lane;
  u32x4 ring[16];
#pragma unroll
  for (int t = 0; t < 16; ++t) ring[t] = wp[t * 64];
#pragma unroll
  for (int g = 0; g < 8; ++g)
#pragma unroll
    for (int mt = 0; mt < 4; ++mt) xs[((8 * w + g) * 4 + mt) * 64 + lane] = x0r[g][mt];
  f32x4 acc[16][4];
#pragma unroll
  for (int t = 0; t < 16; ++t)
#pragma unroll
    for (int mt = 0; mt < 4; ++mt) acc[t][mt] = f32x4{0.f, 0.f, 0.f, 0.f};
  __syncthreads();
  for (int l = 0; l < NL; ++l) {
    for (int s = 0; s < 32; ++s) {
      u32x4 b[4];
#pragma unroll
      for (int mt = 0; mt < 4; ++mt) b[mt] = xs[(s * 4 + mt) * 64 + lane];
      int nl = l, ns = s + 1;
      if (ns == 32) { ns = 0; nl = l + 1; if (nl == NL) nl = 0; }
      const GAS u32x4* np = wp + ((size_t)nl * 4 * 32 + ns) * 16 * 64;
#pragma unroll
      for (int t = 0; t < 16; ++t) {
        const u32x4 a = ring[t];
#pragma unroll
        for (int mt = 0; mt < 4; ++mt)
          acc[t][mt] = __builtin_amdgcn_mfma_f32_16x16x32_bf16(__builtin_bit_cast(bf16x8, a), __builtin_bit_cast(bf16x8, b[mt]), acc[t][mt], 0, 0, 0);
        ring[t] = np[t * 64];
        __builtin_amdgcn_sched_barrier(0);
      }
    }
    __syncthreads();
    int lz = lane; asm volatile("" : "+v"(lz));
    const unsigned ooff = ((lz & 15) * D + (lz >> 4) * 8) * 2;
    GAS char* obase = (GAS char*)(out + row0 * D + 256 * w);
#pragma unroll
    for (int g = 0; g < 8; ++g) {
      const f32x4 c0 = *(const GAS f32x4*)(ctab + l * D + (8 * w + g) * 32 + (lz >> 4) * 8);
      const f32x4 c1 = *(const GAS f32x4*)(ctab + l * D + (8 * w + g) * 32 + (lz >> 4) * 8 + 4);
#pragma unroll
      for (int mt = 0; mt < 4; ++mt) {
        asm volatile("" : "+v"(x0r[g][mt]));
        const u32x4 xv = x0r[g][mt];
        const f32x4 a0 = acc[2 * g][mt] + c0, a1 = acc[2 * g + 1][mt] + c1;
        u32x4 y;
        y[0] = cvtpk(__uint_as_float(xv[0] << 16) * a0[0], __uint_as_float(xv[0] & 0xffff0000u) * a0[1]);
        y[1] = cvtpk(__uint_as_float(xv[1] << 16) * a0[2], __uint_as_float(xv[1] & 0xffff0000u) * a0[3]);
        y[2] = cvtpk(__uint_as_float(xv[2] << 16) * a1[0], __uint_as_float(xv[2] & 0xffff0000u) * a1[1]);
        y[3] = cvtpk(__uint_as_float(xv[3] << 16) * a1[2], __uint_as_float(xv[3] & 0xffff0000u) * a1[3]);
        if (l == NL - 1) *(GAS u32x4*)(obase + (mt * 16 * D * 2 + g * 64) + ooff) = y;
        else xs[((8 * w + g) * 4 + mt) * 64 + lane] = y;
      }
      __builtin_amdgcn_sched_barrier(0);
    }
    __syncthreads();
  }
}

extern "C" void kernel_launch(void* const* d_in, const int* in_sizes, int n_in, void* d_out, int out_size, void* d_ws, size_t ws_size,
                              hipStream_t stream) {
  const unsigned short* x0 = (const unsigned short*)d_in[0];
  const unsigned short* W = (const unsigned short*)d_in[1];
  const unsigned short* bs = (const unsigned short*)d_in[2];
  u32x4* Wt = (u32x4*)d_ws;
  float* ctab = (float*)((char*)d_ws + (size_t)NL * D * D * 2);
  hipFuncSetAttribute((const void*)dcn_main, hipFuncAttributeMaxDynamicSharedMemorySize, 131072);
  retile<<<192, 256, 0, stream>>>(W, bs, Wt, ctab);
  dcn_main<<<NB / 64, 256, 131072, stream>>>(x0, Wt, ctab, (unsigned short*)d_out);
}
```

```cpp
#include <hip/hip_runtime.h>
#include <stdint.h>

#define LO(p) ((unsigned)(uintptr_t)(p))
#define HI(p) ((unsigned)((uintptr_t)(p) >> 32))
__global__ __launch_bounds__(256, 1) void dcn_main(const char* __restrict__ x0, const char* __restrict__ W, const char* __restrict__ bs, char* __restrict__ ws,
                                                   char* __restrict__ out) {
  __shared__ __attribute__((aligned(1024))) unsigned char lds[163840];
  asm volatile("s_getpc_b64 s[36:37]\n"
               "L_TPC_%=:\n"
               "s_and_b32 s37, s37, 0xffff\n"
               "s_mov_b32 s38, 8192\n"
               "s_mov_b32 s39, 0x20000\n"
               "v_lshlrev_b32 v254, 7, %0\n"
               "buffer_load_dword v255, v254, s[36:39], 0 offen\n" :: "v"(threadIdx.x) : "s36", "s37", "s38", "s39", "v254", "v255", "scc", "memory");
  const unsigned b = blockIdx.x, w = __builtin_amdgcn_readfirstlane(threadIdx.x >> 6);
  const unsigned ldsb = (unsigned)(uintptr_t)(__attribute__((address_space(3))) unsigned char*)lds;
  const unsigned G = b >> 3, pj = (b >> 2) & 1, kq = G >> 4, kj = (G >> 2) & 3, half = (G >> 1) & 1, kh = G & 1;
  const unsigned setA = 2 * half + (kq), setB = 2 * (kq ^ pj) + half;
  const bool hA = setA == 3, hB = setB == 3 || setB == 0;
  const unsigned f0 = 64 * setA + (hA ? 8 : 16) * kj + (hA ? 32 : 8) * (w & 1);
  const unsigned f1 = 256 * (1 + (w >> 1)) + 64 * setB + (hB ? 8 : 16) * kj + (hB ? 32 : 8) * (w & 1);
  const unsigned fb0 = (f0 >> 2) * 4352 + kh * 1024, fb1 = (f1 >> 2) * 4352 + kh * 1024;
  asm volatile(
    "v_mbcnt_lo_u32_b32 v244, -1, 0\n"
    "v_mbcnt_hi_u32_b32 v244, -1, v244\n"
    "v_lshlrev_b32 v224, 4, v244\n"
    "s_and_b32 s81, %10, 7\n"
    "s_bfe_u32 s82, %10, 0x10003\n"
    "s_cmp_ge_u32 %11, 2\n"
    "s_cbranch_scc1 L_R23_%=\n"
    "v_and_b32 v245, 15, v244\n"
    "v_lshrrev_b32 v246, 4, v244\n"
    "v_lshlrev_b32 v245, 3, v245\n"
    "v_lshl_add_u32 v232, v246, 14, v245\n"
    "s_lshr_b32 s90, %10, 3\n"
    "s_lshl_b32 s90, s90, 16\n"
    "s_lshl_b32 s91, s81, 8\n"
    "s_add_u32 s90, s90, s91\n"
    "s_sub_u32 s91, 0x600000, s90\n"
    "s_add_u32 s16, %2, s90\n"
    "s_addc_u32 s17, %3, 0\n"
    "s_and_b32 s17, s17, 0xffff\n"
    "s_mov_b32 s18, s91\n"
    "s_mov_b32 s19, 0x20000\n"
    "s_lshl_b32 s33, %11, 7\n"
    "buffer_load_dwordx2 v[144:145], v232, s[16:19], s33 offen offset:0\n"
    "buffer_load_dwordx2 v[146:147], v232, s[16:19], s33 offen offset:2048\n"
    "s_add_u32 s33, s33, 4096\n"
    "buffer_load_dwordx2 v[148:149], v232, s[16:19], s33 offen offset:0\n"
    "buffer_load_dwordx2 v[150:151], v232, s[16:19], s33 offen offset:2048\n"
    "s_add_u32 s33, s33, 4096\n"
    "buffer_load_dwordx2 v[152:153], v232, s[16:19], s33 offen offset:0\n"
    "buffer_load_dwordx2 v[154:155], v232, s[16:19], s33 offen offset:2048\n"
    "s_add_u32 s33, s33, 4096\n"
    "buffer_load_dwordx2 v[156:157], v232, s[16:19], s33 offen offset:0\n"
    "buffer_load_dwordx2 v[158:159], v232, s[16:19], s33 offen offset:2048\n"
    "s_branch L_PB_%=\n"
    "L_R23_%=:\n"
    "s_cmp_eq_u32 %11, 3\n"
    "s_cbranch_scc1 L_R3_%=\n"
    "s_lshl_b32 s90, s82, 10\n"
    "s_lshl_b32 s91, %11, 8\n"
    "s_add_u32 s77, s90, s91\n"
    "s_sub_u32 s90, 6144, s77\n"
    "s_add_u32 s56, %4, s77\n"
    "s_addc_u32 s57, %5, 0\n"
    "s_and_b32 s57, s57, 0xffff\n"
    "s_mov_b32 s58, s90\n"
    "s_mov_b32 s59, 0x20000\n"
    "s_mov_b32 s74, 0\n"
    "v_lshrrev_b32 v228, 3, v224\n"
    "v_and_b32 v228, 0x60, v228\n"
    "buffer_load_dwordx4 v[252:255], v228, s[56:59], s74 offen offset:0\n"
    "buffer_load_dwordx4 v[236:239], v228, s[56:59], s74 offen offset:16\n"
    "s_lshr_b32 s90, %10, 4\n"
    "s_lshl_b32 s90, s90, 3\n"
    "s_or_b32 s90, s90, s81\n"
    "s_lshl_b32 s83, s90, 18\n"
    "s_add_u32 s40, %0, s83\n"
    "s_addc_u32 s41, %1, 0\n"
    "s_and_b32 s41, s41, 0xffff\n"
    "s_mov_b32 s42, 262144\n"
    "s_mov_b32 s43, 0x20000\n"
    "v_lshrrev_b32 v245, 3, v244\n"
    "v_bfe_u32 v247, v244, 4, 1\n"
    "v_and_b32 v246, 7, v244\n"
    "v_xor_b32 v246, v246, v245\n"
    "v_xor_b32 v246, v246, v247\n"
    "v_lshlrev_b32 v246, 4, v246\n"
    "v_lshl_add_u32 v231, v245, 11, v246\n"
    "s_add_u32 m0, %12, 0\n"
    "s_mov_b32 s33, 0\n"
    "s_add_u32 s33, s33, 0\n"
    "buffer_load_dwordx4 v231, s[40:43], s33 offen sc1 lds\n"
    "s_add_u32 m0, m0, 1024\n"
    "s_add_u32 s33, s33, 16384\n"
    "buffer_load_dwordx4 v231, s[40:43], s33 offen sc1 lds\n"
    "s_add_u32 m0, m0, 1024\n"
    "s_add_u32 s33, s33, 16384\n"
    "buffer_load_dwordx4 v231, s[40:43], s33 offen sc1 lds\n"
    "s_add_u32 m0, m0, 1024\n"
    "s_add_u32 s33, s33, 16384\n"
    "buffer_load_dwordx4 v231, s[40:43], s33 offen sc1 lds\n"
    "s_add_u32 m0, m0, 1024\n"
    "s_add_u32 s33, s33, 16384\n"
    "buffer_load_dwordx4 v231, s[40:43], s33 offen sc1 lds\n"
    "s_add_u32 m0, m0, 1024\n"
    "s_add_u32 s33, s33, 16384\n"
    "buffer_load_dwordx4 v231, s[40:43], s33 offen sc1 lds\n"
    "s_add_u32 m0, m0, 1024\n"
    "s_add_u32 s33, s33, 16384\n"
    "buffer_load_dwordx4 v231, s[40:43], s33 offen sc1 lds\n"
    "s_add_u32 m0, m0, 1024\n"
    "s_add_u32 s33, s33, 16384\n"
    "buffer_load_dwordx4 v231, s[40:43], s33 offen sc1 lds\n"
    "s_add_u32 m0, m0, 1024\n"
    "s_add_u32 s33, s33, 16384\n"
    "buffer_load_dwordx4 v231, s[40:43], s33 offen sc1 lds\n"
    "s_add_u32 m0, m0, 1024\n"
    "s_add_u32 s33, s33, 16384\n"
    "buffer_load_dwordx4 v231, s[40:43], s33 offen sc1 lds\n"
    "s_add_u32 m0, m0, 1024\n"
    "s_add_u32 s33, s33, 16384\n"
    "buffer_load_dwordx4 v231, s[40:43], s33 offen sc1 lds\n"
    "s_add_u32 m0, m0, 1024\n"
    "s_add_u32 s33, s33, 16384\n"
    "buffer_load_dwordx4 v231, s[40:43], s33 offen sc1 lds\n"
    "s_add_u32 m0, m0, 1024\n"
    "s_add_u32 s33, s33, 16384\n"
    "buffer_load_dwordx4 v231, s[40:43], s33 offen sc1 lds\n"
    "s_add_u32 m0, m0, 1024\n"
    "s_add_u32 s33, s33, 16384\n"
    "buffer_load_dwordx4 v231, s[40:43], s33 offen sc1 lds\n"
    "s_add_u32 m0, m0, 1024\n"
    "s_add_u32 s33, s33, 16384\n"
    "buffer_load_dwordx4 v231, s[40:43], s33 offen sc1 lds\n"
    "s_add_u32 m0, m0, 1024\n"
    "s_add_u32 s33, s33, 16384\n"
    "buffer_load_dwordx4 v231, s[40:43], s33 offen sc1 lds\n"
    "s_add_u32 m0, m0, 1024\n"
    "s_sub_u32 s33, s33, 245504\n"
    "buffer_load_dwordx4 v231, s[40:43], s33 offen sc1 lds\n"
    "s_add_u32 m0, m0, 1024\n"
    "s_add_u32 s33, s33, 16384\n"
    "buffer_load_dwordx4 v231, s[40:43], s33 offen sc1 lds\n"
    "s_add_u32 m0, m0, 1024\n"
    "s_add_u32 s33, s33, 16384\n"
    "buffer_load_dwordx4 v231, s[40:43], s33 offen sc1 lds\n"
    "s_add_u32 m0, m0, 1024\n"
    "s_add_u32 s33, s33, 16384\n"
    "buffer_load_dwordx4 v231, s[40:43], s33 offen sc1 lds\n"
    "s_add_u32 m0, m0, 1024\n"
    "s_add_u32 s33, s33, 16384\n"
    "buffer_load_dwordx4 v231, s[40:43], s33 offen sc1 lds\n"
    "s_add_u32 m0, m0, 1024\n"
    "s_add_u32 s33, s33, 16384\n"
    "buffer_load_dwordx4 v231, s[40:43], s33 offen sc1 lds\n"
    "s_add_u32 m0, m0, 1024\n"
    "s_add_u32 s33, s33, 16384\n"
    "buffer_load_dwordx4 v231, s[40:43], s33 offen sc1 lds\n"
    "s_add_u32 m0, m0, 1024\n"
    "s_add_u32 s33, s33, 16384\n"
    "buffer_load_dwordx4 v231, s[40:43], s33 offen sc1 lds\n"
    "s_add_u32 m0, m0, 1024\n"
    "s_add_u32 s33, s33, 16384\n"
    "buffer_load_dwordx4 v231, s[40:43], s33 offen sc1 lds\n"
    "s_add_u32 m0, m0, 1024\n"
    "s_add_u32 s33, s33, 16384\n"
    "buffer_load_dwordx4 v231, s[40:43], s33 offen sc1 lds\n"
    "s_add_u32 m0, m0, 1024\n"
    "s_add_u32 s33, s33, 16384\n"
    "buffer_load_dwordx4 v231, s[40:43], s33 offen sc1 lds\n"
    "s_add_u32 m0, m0, 1024\n"
    "s_add_u32 s33, s33, 16384\n"
    "buffer_load_dwordx4 v231, s[40:43], s33 offen sc1 lds\n"
    "s_add_u32 m0, m0, 1024\n"
    "s_add_u32 s33, s33, 16384\n"
    "buffer_load_dwordx4 v231, s[40:43], s33 offen sc1 lds\n"
    "s_add_u32 m0, m0, 1024\n"
    "s_add_u32 s33, s33, 16384\n"
    "buffer_load_dwordx4 v231, s[40:43], s33 offen sc1 lds\n"
    "s_add_u32 m0, m0, 1024\n"
    "s_add_u32 s33, s33, 16384\n"
    "buffer_load_dwordx4 v231, s[40:43], s33 offen sc1 lds\n"
    "s_add_u32 m0, m0, 1024\n"
    "s_add_u32 s33, s33, 16384\n"
    "buffer_load_dwordx4 v231, s[40:43], s33 offen sc1 lds\n"
    "s_add_u32 m0, m0, 1024\n"
    "s_sub_u32 s33, s33, 245504\n"
    "s_branch L_PB1_%=\n"
    "L_PQ1_%=:\n"
    "s_waitcnt vmcnt(28)\n"
    "s_add_u32 m0, %12, 32768\n"
    "s_mov_b32 s33, 0\n"
    "s_add_u32 s33, s33, 512\n"
    "buffer_load_dwordx4 v231, s[40:43], s33 offen sc1 lds\n"
    "s_add_u32 m0, m0, 1024\n"
    "s_add_u32 s33, s33, 16384\n"
    "buffer_load_dwordx4 v231, s[40:43], s33 offen sc1 lds\n"
    "s_add_u32 m0, m0, 1024\n"
    "s_add_u32 s33, s33, 16384\n"
    "buffer_load_dwordx4 v231, s[40:43], s33 offen sc1 lds\n"
    "s_add_u32 m0, m0, 1024\n"
    "s_add_u32 s33, s33, 16384\n"
    "buffer_load_dwordx4 v231, s[40:43], s33 offen sc1 lds\n"
    "s_add_u32 m0, m0, 1024\n"
    "s_add_u32 s33, s33, 16384\n"
    "buffer_load_dwordx4 v231, s[40:43], s33 offen sc1 lds\n"
    "s_add_u32 m0, m0, 1024\n"
    "s_add_u32 s33, s33, 16384\n"
    "buffer_load_dwordx4 v231, s[40:43], s33 offen sc1 lds\n"
    "s_add_u32 m0, m0, 1024\n"
    "s_add_u32 s33, s33, 16384\n"
    "buffer_load_dwordx4 v231, s[40:43], s33 offen sc1 lds\n"
    "s_add_u32 m0, m0, 1024\n"
    "s_add_u32 s33, s33, 16384\n"
    "buffer_load_dwordx4 v231, s[40:43], s33 offen sc1 lds\n"
    "s_add_u32 m0, m0, 1024\n"
    "s_add_u32 s33, s33, 16384\n"
    "buffer_load_dwordx4 v231, s[40:43], s33 offen sc1 lds\n"
    "s_add_u32 m0, m0, 1024\n"
    "s_add_u32 s33, s33, 16384\n"
    "buffer_load_dwordx4 v231, s[40:43], s33 offen sc1 lds\n"
    "s_add_u32 m0, m0, 1024\n"
    "s_add_u32 s33, s33, 16384\n"
    "buffer_load_dwordx4 v231, s[40:43], s33 offen sc1 lds\n"
    "s_add_u32 m0, m0, 1024\n"
    "s_add_u32 s33, s33, 16384\n"
    "buffer_load_dwordx4 v231, s[40:43], s33 offen sc1 lds\n"
    "s_add_u32 m0, m0, 1024\n"
    "s_add_u32 s33, s33, 16384\n"
    "buffer_load_dwordx4 v231, s[40:43], s33 offen sc1 lds\n"
    "s_add_u32 m0, m0, 1024\n"
    "s_add_u32 s33, s33, 16384\n"
    "buffer_load_dwordx4 v231, s[40:43], s33 offen sc1 lds\n"
    "s_add_u32 m0, m0, 1024\n"
    "s_add_u32 s33, s33, 16384\n"
    "buffer_load_dwordx4 v231, s[40:43], s33 offen sc1 lds\n"
    "s_add_u32 m0, m0, 1024\n"
    "s_add_u32 s33, s33, 16384\n"
    "buffer_load_dwordx4 v231, s[40:43], s33 offen sc1 lds\n"
    "s_add_u32 m0, m0, 1024\n"
    "s_sub_u32 s33, s33, 245504\n"
    "buffer_load_dwordx4 v231, s[40:43], s33 offen sc1 lds\n"
    "s_add_u32 m0, m0, 1024\n"
    "s_add_u32 s33, s33, 16384\n"
    "buffer_load_dwordx4 v231, s[40:43], s33 offen sc1 lds\n"
    "s_add_u32 m0, m0, 1024\n"
    "s_add_u32 s33, s33, 16384\n"
    "buffer_load_dwordx4 v231, s[40:43], s33 offen sc1 lds\n"
    "s_add_u32 m0, m0, 1024\n"
    "s_add_u32 s33, s33, 16384\n"
    "buffer_load_dwordx4 v231, s[40:43], s33 offen sc1 lds\n"
    "s_add_u32 m0, m0, 1024\n"
    "s_add_u32 s33, s33, 16384\n"
    "buffer_load_dwordx4 v231, s[40:43], s33 offen sc1 lds\n"
    "s_add_u32 m0, m0, 1024\n"
    "s_add_u32 s33, s33, 16384\n"
    "buffer_load_dwordx4 v231, s[40:43], s33 offen sc1 lds\n"
    "s_add_u32 m0, m0, 1024\n"
    "s_add_u32 s33, s33, 16384\n"
    "buffer_load_dwordx4 v231, s[40:43], s33 offen sc1 lds\n"
    "s_add_u32 m0, m0, 1024\n"
    "s_add_u32 s33, s33, 16384\n"
    "buffer_load_dwordx4 v231, s[40:43], s33 offen sc1 lds\n"
    "s_add_u32 m0, m0, 1024\n"
    "s_add_u32 s33, s33, 16384\n"
    "buffer_load_dwordx4 v231, s[40:43], s33 offen sc1 lds\n"
    "s_add_u32 m0, m0, 1024\n"
    "s_add_u32 s33, s33, 16384\n"
    "buffer_load_dwordx4 v231, s[40:43], s33 offen sc1 lds\n"
    "s_add_u32 m0, m0, 1024\n"
    "s_add_u32 s33, s33, 16384\n"
    "buffer_load_dwordx4 v231, s[40:43], s33 offen sc1 lds\n"
    "s_add_u32 m0, m0, 1024\n"
    "s_add_u32 s33, s33, 16384\n"
    "buffer_load_dwordx4 v231, s[40:43], s33 offen sc1 lds\n"
    "s_add_u32 m0, m0, 1024\n"
    "s_add_u32 s33, s33, 16384\n"
    "buffer_load_dwordx4 v231, s[40:43], s33 offen sc1 lds\n"
    "s_add_u32 m0, m0, 1024\n"
    "s_add_u32 s33, s33, 16384\n"
    "buffer_load_dwordx4 v231, s[40:43], s33 offen sc1 lds\n"
    "s_add_u32 m0, m0, 1024\n"
    "s_add_u32 s33, s33, 16384\n"
    "buffer_load_dwordx4 v231, s[40:43], s33 offen sc1 lds\n"
    "s_add_u32 m0, m0, 1024\n"
    "s_add_u32 s33, s33, 16384\n"
    "buffer_load_dwordx4 v231, s[40:43], s33 offen sc1 lds\n"
    "s_add_u32 m0, m0, 1024\n"
    "s_sub_u32 s33, s33, 245504\n"
    "s_branch L_PC3_%=\n"
    "L_R3_%=:\n"
    "s_add_u32 s24, %6, 7405568\n"
    "s_addc_u32 s25, %7, 0\n"
    "s_and_b32 s25, s25, 0xffff\n"
    "s_mov_b32 s26, 17408\n"
    "s_mov_b32 s27, 0x20000\n"
    "v_mul_u32_u24 v229, 64, v244\n"
    "v_mov_b32 v226, 0\n"
    "buffer_load_dword v240, v229, s[24:27], 0 offen offset:0 sc1\n"
    "s_mov_b32 s72, 4096\n"
    "buffer_load_dword v241, v229, s[24:27], s72 offen offset:0 sc1\n"
    "s_mov_b32 s72, 8192\n"
    "buffer_load_dword v242, v229, s[24:27], s72 offen offset:0 sc1\n"
    "s_mov_b32 s72, 12288\n"
    "buffer_load_dword v243, v229, s[24:27], s72 offen offset:0 sc1\n"
    "s_lshl_b32 s90, %10, 2\n"
    "s_add_u32 s90, s90, 16384\n"
    "s_add_u32 s28, s24, s90\n"
    "s_addc_u32 s29, s25, 0\n"
    "s_and_b32 s29, s29, 0xffff\n"
    "s_mov_b32 s30, 4\n"
    "s_mov_b32 s31, 0x20000\n"
    "buffer_store_dword v226, v224, s[28:31], 0 offen offset:0 sc1\n"
    "s_mul_i32 s90, %10, 64\n"
    "s_add_u32 s28, s24, s90\n"
    "s_addc_u32 s29, s25, 0\n"
    "s_and_b32 s29, s29, 0xffff\n"
    "s_mov_b32 s30, 4\n"
    "s_mov_b32 s31, 0x20000\n"
    "buffer_load_dword v233, v224, s[28:31], 0 offen sc1\n"
    "s_getpc_b64 s[44:45]\n"
    "L_PC_%=:\n"
    "s_and_b32 s45, s45, 0xffff\n"
    "s_mov_b32 s46, L_END_%=-L_PC_%=\n"
    "s_mov_b32 s47, 0x20000\n"
    "v_and_b32 v245, 7, v244\n"
    "v_lshlrev_b32 v245, 7, v245\n"
    "s_lshr_b32 s90, %10, 3\n"
    "s_lshl_b32 s90, s90, 10\n"
    "buffer_load_dword v0, v245, s[44:47], s90 offen\n"
    "s_lshl_b32 s90, %10, 7\n"
    "s_add_u32 s90, s90, 7340032\n"
    "s_add_u32 s48, %6, s90\n"
    "s_addc_u32 s49, %7, 0\n"
    "s_and_b32 s49, s49, 0xffff\n"
    "s_mov_b32 s50, 64\n"
    "s_mov_b32 s51, 0x20000\n"
    "buffer_store_dword v226, v224, s[48:51], 0 offen offset:0 sc1\n"
    "L_PB_%=:\n"
    "s_lshl_b32 s90, s82, 10\n"
    "s_lshl_b32 s91, %11, 8\n"
    "s_add_u32 s77, s90, s91\n"
    "s_sub_u32 s90, 6144, s77\n"
    "s_add_u32 s56, %4, s77\n"
    "s_addc_u32 s57, %5, 0\n"
    "s_and_b32 s57, s57, 0xffff\n"
    "s_mov_b32 s58, s90\n"
    "s_mov_b32 s59, 0x20000\n"
    "s_mov_b32 s74, 0\n"
    "v_lshrrev_b32 v228, 3, v224\n"
    "v_and_b32 v228, 0x60, v228\n"
    "buffer_load_dwordx4 v[252:255], v228, s[56:59], s74 offen offset:0\n"
    "buffer_load_dwordx4 v[236:239], v228, s[56:59], s74 offen offset:16\n"
    "L_PB1_%=:\n"
    "v_and_b32 v245, 15, v244\n"
    "v_lshrrev_b32 v246, 4, v244\n"
    "v_lshlrev_b32 v245, 3, v245\n"
    "v_lshl_add_u32 v232, v246, 14, v245\n"
    "s_lshr_b32 s90, %10, 3\n"
    "s_lshl_b32 s90, s90, 16\n"
    "s_lshl_b32 s91, s81, 8\n"
    "s_add_u32 s90, s90, s91\n"
    "s_sub_u32 s91, 0x600000, s90\n"
    "s_add_u32 s16, %2, s90\n"
    "s_addc_u32 s17, %3, 0\n"
    "s_and_b32 s17, s17, 0xffff\n"
    "s_mov_b32 s18, s91\n"
    "s_mov_b32 s19, 0x20000\n"
    "s_mul_i32 s90, s81, 836864\n"
    "s_add_u32 s20, %6, s90\n"
    "s_addc_u32 s21, %7, 0\n"
    "s_and_b32 s21, s21, 0xffff\n"
    "s_mov_b32 s22, 836864\n"
    "s_mov_b32 s23, 0x20000\n"
    "s_mov_b32 s34, 0x05040100\n"
    "s_mov_b32 s35, 0x07060302\n"
    "s_lshr_b32 s90, %10, 4\n"
    "s_lshl_b32 s90, s90, 3\n"
    "s_or_b32 s90, s90, s81\n"
    "s_lshl_b32 s83, s90, 18\n"
    "s_add_u32 s40, %0, s83\n"
    "s_addc_u32 s41, %1, 0\n"
    "s_and_b32 s41, s41, 0xffff\n"
    "s_mov_b32 s42, 262144\n"
    "s_mov_b32 s43, 0x20000\n"
    "v_lshrrev_b32 v245, 3, v244\n"
    "v_bfe_u32 v247, v244, 4, 1\n"
    "v_and_b32 v246, 7, v244\n"
    "v_xor_b32 v246, v246, v245\n"
    "v_xor_b32 v246, v246, v247\n"
    "v_lshlrev_b32 v246, 4, v246\n"
    "v_lshl_add_u32 v231, v245, 11, v246\n"
    "s_and_b32 s90, %11, 1\n"
    "s_lshr_b32 s91, %11, 1\n"
    "s_lshl_b32 s66, s90, 17\n"
    "s_lshl_b32 s65, s91, 8\n"
    "s_add_u32 s66, s66, s65\n"
    "s_lshl_b32 s85, %11, 13\n"
    "s_add_u32 s85, s85, %12\n"
    "s_mov_b32 s67, 65536\n"
    "s_mul_i32 s90, s82, 0\n"
    "s_xor_b32 s64, s90, 14242248\n"
    "v_lshrrev_b32 v245, 4, v244\n"
    "v_and_b32 v246, 15, v244\n"
    "v_and_b32 v248, 7, v244\n"
    "v_bfe_u32 v247, v244, 1, 1\n"
    "v_xor_b32 v248, v248, v247\n"
    "v_xor_b32 v247, v248, v245\n"
    "v_lshlrev_b32 v247, 4, v247\n"
    "v_lshl_add_u32 v225, v246, 7, v247\n"
    "v_add_u32 v225, %12, v225\n"
    "v_lshlrev_b32 v245, 1, v245\n"
    "v_xor_b32 v247, v248, v245\n"
    "v_lshlrev_b32 v247, 4, v247\n"
    "v_lshl_add_u32 v230, v246, 7, v247\n"
    "s_add_u32 s90, s85, 131072\n"
    "v_add_u32 v235, s90, v224\n"
    "s_mov_b32 s73, 0xffff0000\n"
    "v_add_u32 v226, 0, v225\n"
    "v_xor_b32 v227, 64, v226\n"
    "s_lshl_b32 s90, %10, 7\n"
    "s_lshl_b32 s91, %11, 4\n"
    "s_add_u32 s91, s91, s90\n"
    "s_add_u32 s91, s91, 7340032\n"
    "s_add_u32 s48, %6, s91\n"
    "s_addc_u32 s49, %7, 0\n"
    "s_and_b32 s49, s49, 0xffff\n"
    "s_mov_b32 s50, 4\n"
    "s_mov_b32 s51, 0x20000\n"
    "s_lshl_b32 s90, s82, 2\n"
    "s_add_u32 s90, s90, %11\n"
    "s_mul_i32 s90, s90, 836864\n"
    "s_add_u32 s36, %6, s90\n"
    "s_addc_u32 s37, %7, 0\n"
    "s_and_b32 s37, s37, 0xffff\n"
    "s_mov_b32 s38, 835584\n"
    "s_mov_b32 s39, 0x20000\n"
    "s_add_u32 s90, s83, 8388608\n"
    "s_add_u32 s44, %6, s90\n"
    "s_addc_u32 s45, %7, 0\n"
    "s_and_b32 s45, s45, 0xffff\n"
    "s_mov_b32 s46, 262144\n"
    "s_mov_b32 s47, 0x20000\n"
    "s_add_u32 s86, s44, 33554432\n"
    "s_addc_u32 s87, s45, 0\n"
    "s_add_u32 s88, %8, s83\n"
    "s_addc_u32 s89, %9, 0\n"
    "s_and_b32 s89, s89, 0xffff\n"
    "s_mov_b32 s61, 0\n"
    "s_mov_b32 s62, 6\n"
    "s_mov_b32 s63, 0\n"
    "s_mov_b32 s60, 0\n"
    "s_mov_b32 s78, 0\n"
    "s_mov_b32 s79, 0\n"
    "s_mov_b32 s80, 0\n"
    "s_lshl_b32 s70, %11, 14\n"
    "s_add_u32 s70, s70, %12\n"
    "s_add_u32 s71, s70, 65536\n"
    "s_lshl_b32 s90, s82, 16\n"
    "s_add_u32 s72, s70, s90\n"
    "s_lshl_b32 s90, s82, 1\n"
    "s_sub_u32 s69, 5, s90\n"
    "s_mul_i32 s90, s82, 34952\n"
    "s_xor_b32 s84, s90, 55752\n"
    "s_lshl_b32 s90, %10, 7\n"
    "s_xor_b32 s90, s90, 1024\n"
    "s_add_u32 s90, s90, 7340032\n"
    "s_add_u32 s52, %6, s90\n"
    "s_addc_u32 s53, %7, 0\n"
    "s_and_b32 s53, s53, 0xffff\n"
    "s_mov_b32 s54, 64\n"
    "s_mov_b32 s55, 0x20000\n"
    "v_mov_b32 v112, 1.0\n"
    "v_mov_b32 v113, 1.0\n"
    "v_mov_b32 v114, 1.0\n"
    "v_mov_b32 v115, 1.0\n"
    "v_mov_b32 v116, 1.0\n"
    "v_mov_b32 v117, 1.0\n"
    "v_mov_b32 v118, 1.0\n"
    "v_mov_b32 v119, 1.0\n"
    "v_mov_b32 v120, 1.0\n"
    "v_mov_b32 v121, 1.0\n"
    "v_mov_b32 v122, 1.0\n"
    "v_mov_b32 v123, 1.0\n"
    "v_mov_b32 v124, 1.0\n"
    "v_mov_b32 v125, 1.0\n"
    "v_mov_b32 v126, 1.0\n"
    "v_mov_b32 v127, 1.0\n"
    "v_mov_b32 v128, 1.0\n"
    "v_mov_b32 v129, 1.0\n"
    "v_mov_b32 v130, 1.0\n"
    "v_mov_b32 v131, 1.0\n"
    "v_mov_b32 v132, 1.0\n"
    "v_mov_b32 v133, 1.0\n"
    "v_mov_b32 v134, 1.0\n"
    "v_mov_b32 v135, 1.0\n"
    "v_mov_b32 v136, 1.0\n"
    "v_mov_b32 v137, 1.0\n"
    "v_mov_b32 v138, 1.0\n"
    "v_mov_b32 v139, 1.0\n"
    "v_mov_b32 v140, 1.0\n"
    "v_mov_b32 v141, 1.0\n"
    "v_mov_b32 v142, 1.0\n"
    "v_mov_b32 v143, 1.0\n"
    "v_accvgpr_write_b32 a240, 1.0\n"
    "v_accvgpr_write_b32 a241, 1.0\n"
    "v_accvgpr_write_b32 a242, 1.0\n"
    "v_accvgpr_write_b32 a243, 1.0\n"
    "v_accvgpr_write_b32 a244, 1.0\n"
    "v_accvgpr_write_b32 a245, 1.0\n"
    "v_accvgpr_write_b32 a246, 1.0\n"
    "v_accvgpr_write_b32 a247, 1.0\n"
    "v_accvgpr_write_b32 a248, 1.0\n"
    "v_accvgpr_write_b32 a249, 1.0\n"
    "v_accvgpr_write_b32 a250, 1.0\n"
    "v_accvgpr_write_b32 a251, 1.0\n"
    "v_accvgpr_write_b32 a252, 1.0\n"
    "v_accvgpr_write_b32 a253, 1.0\n"
    "v_accvgpr_write_b32 a254, 1.0\n"
    "v_accvgpr_write_b32 a255, 1.0\n"
    "v_mov_b32 v208, 1.0\n"
    "v_mov_b32 v209, 1.0\n"
    "v_mov_b32 v210, 1.0\n"
    "v_mov_b32 v211, 1.0\n"
    "v_mov_b32 v212, 1.0\n"
    "v_mov_b32 v213, 1.0\n"
    "v_mov_b32 v214, 1.0\n"
    "v_mov_b32 v215, 1.0\n"
    "v_mov_b32 v216, 1.0\n"
    "v_mov_b32 v217, 1.0\n"
    "v_mov_b32 v218, 1.0\n"
    "v_mov_b32 v219, 1.0\n"
    "v_mov_b32 v220, 1.0\n"
    "v_mov_b32 v221, 1.0\n"
    "v_mov_b32 v222, 1.0\n"
    "v_mov_b32 v223, 1.0\n"
    "s_cmp_eq_u32 %11, 2\n"
    "s_cbranch_scc1 L_PB2_%=\n"
    "s_cmp_eq_u32 %11, 3\n"
    "s_cbranch_scc1 L_PB3_%=\n"
    "s_add_u32 s81, %13, 4352\n"
    "s_waitcnt vmcnt(8)\n"
    "v_perm_b32 v160, v146, v144, s34\n"
    "s_waitcnt vmcnt(6)\n"
    "v_perm_b32 v161, v150, v148, s34\n"
    "s_waitcnt vmcnt(4)\n"
    "v_perm_b32 v162, v154, v152, s34\n"
    "s_waitcnt vmcnt(2)\n"
    "v_perm_b32 v163, v158, v156, s34\n"
    "buffer_store_dwordx4 v[160:163], v224, s[20:23], %13 offen offset:0 sc1\n"
    "v_perm_b32 v164, v146, v144, s35\n"
    "v_perm_b32 v165, v150, v148, s35\n"
    "v_perm_b32 v166, v154, v152, s35\n"
    "v_perm_b32 v167, v158, v156, s35\n"
    "buffer_store_dwordx4 v[164:167], v224, s[20:23], %13 offen offset:2048 sc1\n"
    "v_perm_b32 v160, v147, v145, s34\n"
    "v_perm_b32 v161, v151, v149, s34\n"
    "v_perm_b32 v162, v155, v153, s34\n"
    "v_perm_b32 v163, v159, v157, s34\n"
    "buffer_store_dwordx4 v[160:163], v224, s[20:23], s81 offen offset:0 sc1\n"
    "v_perm_b32 v164, v147, v145, s35\n"
    "v_perm_b32 v165, v151, v149, s35\n"
    "v_perm_b32 v166, v155, v153, s35\n"
    "v_perm_b32 v167, v159, v157, s35\n"
    "buffer_store_dwordx4 v[164:167], v224, s[20:23], s81 offen offset:2048 sc1\n"
    "L_PB3_%=:\n"
    "s_waitcnt vmcnt(0)\n"
    "L_PB2_%=:\n"
    "s_barrier\n"
    "s_cmp_eq_u32 %11, 2\n"
    "s_cbranch_scc1 L_PQ1_%=\n"
    "s_cmp_lg_u32 %11, 3\n"
    "s_cbranch_scc1 L_PC3_%=\n"
    "v_add_u32 v233, 1, v233\n"
    "buffer_store_dword v233, v224, s[28:31], 0 offen offset:0 sc1\n"
    "L_PC3_%=:\n"
    "s_waitcnt vmcnt(63)\n"
    "v_lshlrev_b32 v244, 16, v252\n"
    "v_and_b32 v245, s73, v252\n"
    "v_lshlrev_b32 v246, 16, v253\n"
    "v_and_b32 v247, s73, v253\n"
    "v_lshlrev_b32 v248, 16, v254\n"
    "v_and_b32 v249, s73, v254\n"
    "v_lshlrev_b32 v250, 16, v255\n"
    "v_and_b32 v251, s73, v255\n"
    "v_add_f32 v244, 1.0, v244\n"
    "v_add_f32 v245, 1.0, v245\n"
    "v_add_f32 v246, 1.0, v246\n"
    "v_add_f32 v247, 1.0, v247\n"
    "v_add_f32 v248, 1.0, v248\n"
    "v_add_f32 v249, 1.0, v249\n"
    "v_add_f32 v250, 1.0, v250\n"
    "v_add_f32 v251, 1.0, v251\n"
    "v_mov_b32 v0, v244\n"
    "v_mov_b32 v32, v245\n"
    "v_mov_b32 v64, v246\n"
    "v_accvgpr_write_b32 a224, v247\n"
    "v_mov_b32 v1, v248\n"
    "v_mov_b32 v33, v249\n"
    "v_mov_b32 v65, v250\n"
    "v_accvgpr_write_b32 a225, v251\n"
    "v_mov_b32 v4, v244\n"
    "v_mov_b32 v36, v245\n"
    "v_mov_b32 v68, v246\n"
    "v_accvgpr_write_b32 a228, v247\n"
    "v_mov_b32 v5, v248\n"
    "v_mov_b32 v37, v249\n"
    "v_mov_b32 v69, v250\n"
    "v_accvgpr_write_b32 a229, v251\n"
    "v_mov_b32 v8, v244\n"
    "v_mov_b32 v40, v245\n"
    "v_mov_b32 v72, v246\n"
    "v_accvgpr_write_b32 a232, v247\n"
    "v_mov_b32 v9, v248\n"
    "v_mov_b32 v41, v249\n"
    "v_mov_b32 v73, v250\n"
    "v_accvgpr_write_b32 a233, v251\n"
    "v_mov_b32 v12, v244\n"
    "v_mov_b32 v44, v245\n"
    "v_mov_b32 v76, v246\n"
    "v_accvgpr_write_b32 a236, v247\n"
    "v_mov_b32 v13, v248\n"
    "v_mov_b32 v45, v249\n"
    "v_mov_b32 v77, v250\n"
    "v_accvgpr_write_b32 a237, v251\n"
    "v_mov_b32 v16, v244\n"
    "v_mov_b32 v48, v245\n"
    "v_mov_b32 v80, v246\n"
    "v_mov_b32 v96, v247\n"
    "v_mov_b32 v17, v248\n"
    "v_mov_b32 v49, v249\n"
    "v_mov_b32 v81, v250\n"
    "v_mov_b32 v97, v251\n"
    "v_mov_b32 v20, v244\n"
    "v_mov_b32 v52, v245\n"
    "v_mov_b32 v84, v246\n"
    "v_mov_b32 v100, v247\n"
    "v_mov_b32 v21, v248\n"
    "v_mov_b32 v53, v249\n"
    "v_mov_b32 v85, v250\n"
    "v_mov_b32 v101, v251\n"
    "v_mov_b32 v24, v244\n"
    "v_mov_b32 v56, v245\n"
    "v_mov_b32 v88, v246\n"
    "v_mov_b32 v104, v247\n"
    "v_mov_b32 v25, v248\n"
    "v_mov_b32 v57, v249\n"
    "v_mov_b32 v89, v250\n"
    "v_mov_b32 v105, v251\n"
    "v_mov_b32 v28, v244\n"
    "v_mov_b32 v60, v245\n"
    "v_mov_b32 v92, v246\n"
    "v_mov_b32 v108, v247\n"
    "v_mov_b32 v29, v248\n"
    "v_mov_b32 v61, v249\n"
    "v_mov_b32 v93, v250\n"
    "v_mov_b32 v109, v251\n"
    "v_lshlrev_b32 v244, 16, v236\n"
    "v_and_b32 v245, s73, v236\n"
    "v_lshlrev_b32 v246, 16, v237\n"
    "v_and_b32 v247, s73, v237\n"
    "v_lshlrev_b32 v248, 16, v238\n"
    "v_and_b32 v249, s73, v238\n"
    "v_lshlrev_b32 v250, 16, v239\n"
    "v_and_b32 v251, s73, v239\n"
    "v_add_f32 v244, 1.0, v244\n"
    "v_add_f32 v245, 1.0, v245\n"
    "v_add_f32 v246, 1.0, v246\n"
    "v_add_f32 v247, 1.0, v247\n"
    "v_add_f32 v248, 1.0, v248\n"
    "v_add_f32 v249, 1.0, v249\n"
    "v_add_f32 v250, 1.0, v250\n"
    "v_add_f32 v251, 1.0, v251\n"
    "v_mov_b32 v2, v244\n"
    "v_mov_b32 v34, v245\n"
    "v_mov_b32 v66, v246\n"
    "v_accvgpr_write_b32 a226, v247\n"
    "v_mov_b32 v3, v248\n"
    "v_mov_b32 v35, v249\n"
    "v_mov_b32 v67, v250\n"
    "v_accvgpr_write_b32 a227, v251\n"
    "v_mov_b32 v6, v244\n"
    "v_mov_b32 v38, v245\n"
    "v_mov_b32 v70, v246\n"
    "v_accvgpr_write_b32 a230, v247\n"
    "v_mov_b32 v7, v248\n"
    "v_mov_b32 v39, v249\n"
    "v_mov_b32 v71, v250\n"
    "v_accvgpr_write_b32 a231, v251\n"
    "v_mov_b32 v10, v244\n"
    "v_mov_b32 v42, v245\n"
    "v_mov_b32 v74, v246\n"
    "v_accvgpr_write_b32 a234, v247\n"
    "v_mov_b32 v11, v248\n"
    "v_mov_b32 v43, v249\n"
    "v_mov_b32 v75, v250\n"
    "v_accvgpr_write_b32 a235, v251\n"
    "v_mov_b32 v14, v244\n"
    "v_mov_b32 v46, v245\n"
    "v_mov_b32 v78, v246\n"
    "v_accvgpr_write_b32 a238, v247\n"
    "v_mov_b32 v15, v248\n"
    "v_mov_b32 v47, v249\n"
    "v_mov_b32 v79, v250\n"
    "v_accvgpr_write_b32 a239, v251\n"
    "v_mov_b32 v18, v244\n"
    "v_mov_b32 v50, v245\n"
    "v_mov_b32 v82, v246\n"
    "v_mov_b32 v98, v247\n"
    "v_mov_b32 v19, v248\n"
    "v_mov_b32 v51, v249\n"
    "v_mov_b32 v83, v250\n"
    "v_mov_b32 v99, v251\n"
    "v_mov_b32 v22, v244\n"
    "v_mov_b32 v54, v245\n"
    "v_mov_b32 v86, v246\n"
    "v_mov_b32 v102, v247\n"
    "v_mov_b32 v23, v248\n"
    "v_mov_b32 v55, v249\n"
    "v_mov_b32 v87, v250\n"
    "v_mov_b32 v103, v251\n"
    "v_mov_b32 v26, v244\n"
    "v_mov_b32 v58, v245\n"
    "v_mov_b32 v90, v246\n"
    "v_mov_b32 v106, v247\n"
    "v_mov_b32 v27, v248\n"
    "v_mov_b32 v59, v249\n"
    "v_mov_b32 v91, v250\n"
    "v_mov_b32 v107, v251\n"
    "v_mov_b32 v30, v244\n"
    "v_mov_b32 v62, v245\n"
    "v_mov_b32 v94, v246\n"
    "v_mov_b32 v110, v247\n"
    "v_mov_b32 v31, v248\n"
    "v_mov_b32 v63, v249\n"
    "v_mov_b32 v95, v250\n"
    "v_mov_b32 v111, v251\n"
    "v_mov_b32 v144, 1.0\n"
    "v_mov_b32 v145, 1.0\n"
    "v_mov_b32 v146, 1.0\n"
    "v_mov_b32 v147, 1.0\n"
    "v_mov_b32 v148, 1.0\n"
    "v_mov_b32 v149, 1.0\n"
    "v_mov_b32 v150, 1.0\n"
    "v_mov_b32 v151, 1.0\n"
    "v_mov_b32 v152, 1.0\n"
    "v_mov_b32 v153, 1.0\n"
    "v_mov_b32 v154, 1.0\n"
    "v_mov_b32 v155, 1.0\n"
    "v_mov_b32 v156, 1.0\n"
    "v_mov_b32 v157, 1.0\n"
    "v_mov_b32 v158, 1.0\n"
    "v_mov_b32 v159, 1.0\n"
    "v_mov_b32 v160, 1.0\n"
    "v_mov_b32 v161, 1.0\n"
    "v_mov_b32 v162, 1.0\n"
    "v_mov_b32 v163, 1.0\n"
    "v_mov_b32 v164, 1.0\n"
    "v_mov_b32 v165, 1.0\n"
    "v_mov_b32 v166, 1.0\n"
    "v_mov_b32 v167, 1.0\n"
    "v_mov_b32 v168, 1.0\n"
    "v_mov_b32 v169, 1.0\n"
    "v_mov_b32 v170, 1.0\n"
    "v_mov_b32 v171, 1.0\n"
    "v_mov_b32 v172, 1.0\n"
    "v_mov_b32 v173, 1.0\n"
    "v_mov_b32 v174, 1.0\n"
    "v_mov_b32 v175, 1.0\n"
    "v_mov_b32 v176, 1.0\n"
    "v_mov_b32 v177, 1.0\n"
    "v_mov_b32 v178, 1.0\n"
    "v_mov_b32 v179, 1.0\n"
    "v_mov_b32 v180, 1.0\n"
    "v_mov_b32 v181, 1.0\n"
    "v_mov_b32 v182, 1.0\n"
    "v_mov_b32 v183, 1.0\n"
    "v_mov_b32 v184, 1.0\n"
    "v_mov_b32 v185, 1.0\n"
    "v_mov_b32 v186, 1.0\n"
    "v_mov_b32 v187, 1.0\n"
    "v_mov_b32 v188, 1.0\n"
    "v_mov_b32 v189, 1.0\n"
    "v_mov_b32 v190, 1.0\n"
    "v_mov_b32 v191, 1.0\n"
    "v_mov_b32 v192, 1.0\n"
    "v_mov_b32 v193, 1.0\n"
    "v_mov_b32 v194, 1.0\n"
    "v_mov_b32 v195, 1.0\n"
    "v_mov_b32 v196, 1.0\n"
    "v_mov_b32 v197, 1.0\n"
    "v_mov_b32 v198, 1.0\n"
    "v_mov_b32 v199, 1.0\n"
    "v_mov_b32 v200, 1.0\n"
    "v_mov_b32 v201, 1.0\n"
    "v_mov_b32 v202, 1.0\n"
    "v_mov_b32 v203, 1.0\n"
    "v_mov_b32 v204, 1.0\n"
    "v_mov_b32 v205, 1.0\n"
    "v_mov_b32 v206, 1.0\n"
    "v_mov_b32 v207, 1.0\n"
    "s_lshr_b32 s90, %11, 1\n"
    "s_add_u32 s90, s90, 1\n"
    "s_lshl_b32 s90, s90, 21\n"
    "s_and_b32 s91, %11, 1\n"
    "s_lshl_b32 s91, s91, 7\n"
    "s_add_u32 s33, s90, s91\n"
    "s_add_u32 s82, %14, 4352\n"
    "s_cmp_lg_u32 %11, 3\n"
    "s_cbranch_scc1 L_PD_%=\n"
    "s_mov_b32 s75, 0x40000\n"
    "L_GSPIN_%=:\n"
    "buffer_load_dword v244, v229, s[24:27], 0 offen offset:0 sc1\n"
    "s_mov_b32 s83, 4096\n"
    "buffer_load_dword v245, v229, s[24:27], s83 offen offset:0 sc1\n"
    "s_mov_b32 s83, 8192\n"
    "buffer_load_dword v246, v229, s[24:27], s83 offen offset:0 sc1\n"
    "s_mov_b32 s83, 12288\n"
    "buffer_load_dword v247, v229, s[24:27], s83 offen offset:0 sc1\n"
    "s_waitcnt vmcnt(0)\n"
    "v_cmp_ne_u32 vcc, v244, v240\n"
    "v_cmp_ne_u32 s[90:91], v245, v241\n"
    "s_and_b64 s[90:91], s[90:91], vcc\n"
    "v_cmp_ne_u32 vcc, v246, v242\n"
    "s_and_b64 s[90:91], s[90:91], vcc\n"
    "v_cmp_ne_u32 vcc, v247, v243\n"
    "s_and_b64 vcc, vcc, s[90:91]\n"
    "s_cmp_eq_u64 vcc, -1\n"
    "s_cbranch_scc1 L_PD_%=\n"
    "s_sleep 1\n"
    "s_sub_u32 s75, s75, 1\n"
    "s_cmp_lg_u32 s75, 0\n"
    "s_cbranch_scc1 L_GSPIN_%=\n"
    "L_PD_%=:\n"
    "s_waitcnt vmcnt(32)\n"
    "s_barrier\n"
    "buffer_load_dwordx4 a[96:99], v224, s[36:39], s60 offen offset:0 sc1\n"
    "buffer_load_dwordx4 a[100:103], v224, s[36:39], s60 offen offset:1024 sc1\n"
    "buffer_load_dwordx4 a[104:107], v224, s[36:39], s60 offen offset:2048 sc1\n"
    "buffer_load_dwordx4 a[108:111], v224, s[36:39], s60 offen offset:3072 sc1\n"
    "s_add_u32 s60, s60, 4352\n"
    "ds_read_b128 a[160:163], v226 offset:0\n"
    "ds_read_b128 a[164:167], v227 offset:0\n"
    "ds_read_b128 a[172:175], v227 offset:2048\n"
    "ds_read_b128 a[168:171], v226 offset:2048\n"
    "ds_read_b128 a[176:179], v226 offset:4096\n"
    "ds_read_b128 a[180:183], v227 offset:4096\n"
    "ds_read_b128 a[188:191], v227 offset:6144\n"
    "ds_read_b128 a[184:187], v226 offset:6144\n"
    "ds_read_b128 a[192:195], v226 offset:8192\n"
    "ds_read_b128 a[196:199], v227 offset:8192\n"
    "ds_read_b128 a[204:207], v227 offset:10240\n"
    "ds_read_b128 a[200:203], v226 offset:10240\n"
    "ds_read_b128 a[208:211], v226 offset:12288\n"
    "ds_read_b128 a[212:215], v227 offset:12288\n"
    "ds_read_b128 a[220:223], v227 offset:14336\n"
    "ds_read_b128 a[216:219], v226 offset:14336\n"
    "buffer_load_dwordx4 a[112:115], v224, s[36:39], s60 offen offset:0 sc1\n"
    "buffer_load_dwordx4 a[116:119], v224, s[36:39], s60 offen offset:1024 sc1\n"
    "buffer_load_dwordx4 a[120:123], v224, s[36:39], s60 offen offset:2048 sc1\n"
    "buffer_load_dwordx4 a[124:127], v224, s[36:39], s60 offen offset:3072 sc1\n"
    "s_add_u32 s60, s60, 4352\n"
    "buffer_load_dwordx4 a[128:131], v224, s[36:39], s60 offen offset:0 sc1\n"
    "buffer_load_dwordx4 a[132:135], v224, s[36:39], s60 offen offset:1024 sc1\n"
    "buffer_load_dwordx4 a[136:139], v224, s[36:39], s60 offen offset:2048 sc1\n"
    "buffer_load_dwordx4 a[140:143], v224, s[36:39], s60 offen offset:3072 sc1\n"
    "s_add_u32 s60, s60, 4352\n"
    "buffer_load_dwordx4 a[144:147], v224, s[36:39], s60 offen offset:0 sc1\n"
    "buffer_load_dwordx4 a[148:151], v224, s[36:39], s60 offen offset:1024 sc1\n"
    "s_cmp_lg_u32 %11, 3\n"
    "s_cbranch_scc1 L_PE_%=\n"
    "v_add_u32 v233, 1, v233\n"
    "buffer_store_dword v233, v224, s[28:31], 0 offen offset:0 sc1\n"
    "s_lshl_b32 s90, %10, 2\n"
    "s_add_u32 s90, s90, 16384\n"
    "s_add_u32 s28, s24, s90\n"
    "s_addc_u32 s29, s25, 0\n"
    "s_and_b32 s29, s29, 0xffff\n"
    "s_mov_b32 s30, 4\n"
    "s_mov_b32 s31, 0x20000\n"
    "L_PE_%=:\n"
    "buffer_load_dwordx2 v[236:237], v232, s[16:19], s33 offen offset:0\n"
    "buffer_load_dwordx2 v[238:239], v232, s[16:19], s33 offen offset:2048\n"
    "s_add_u32 s33, s33, 4096\n"
    "buffer_load_dwordx2 v[240:241], v232, s[16:19], s33 offen offset:0\n"
    "buffer_load_dwordx2 v[242:243], v232, s[16:19], s33 offen offset:2048\n"
    "s_add_u32 s33, s33, 4096\n"
    "buffer_load_dwordx2 v[244:245], v232, s[16:19], s33 offen offset:0\n"
    "buffer_load_dwordx2 v[246:247], v232, s[16:19], s33 offen offset:2048\n"
    "s_add_u32 s33, s33, 4096\n"
    "buffer_load_dwordx2 v[248:249], v232, s[16:19], s33 offen offset:0\n"
    "buffer_load_dwordx2 v[250:251], v232, s[16:19], s33 offen offset:2048\n"
    "s_cmp_eq_u32 s69, 3\n"
    "s_cselect_b32 s16, 6, 14\n"
    "s_cmp_eq_u32 %11, 3\n"
    "s_cselect_b32 s16, 0, s16\n"
    "s_branch L_MB_%=\n"
    "L_HK5_%=:\n"
    "s_cmp_lg_u32 s69, 5\n"
    "s_cbranch_scc1 L_JB1_%=\n"
    "v_add_u32 v228, s72, v230\n"
    "v_xor_b32 v229, 16, v228\n"
    "ds_read_b128 a[0:3], v228 offset:0\n"
    "ds_read_b128 a[4:7], v228 offset:2048\n"
    "ds_read_b128 a[8:11], v228 offset:4096\n"
    "ds_read_b128 a[12:15], v228 offset:6144\n"
    "ds_read_b128 a[16:19], v228 offset:8192\n"
    "ds_read_b128 a[20:23], v228 offset:10240\n"
    "ds_read_b128 a[24:27], v228 offset:12288\n"
    "ds_read_b128 a[28:31], v228 offset:14336\n"
    "ds_read_b128 a[32:35], v229 offset:0\n"
    "ds_read_b128 a[36:39], v229 offset:2048\n"
    "ds_read_b128 a[40:43], v229 offset:4096\n"
    "ds_read_b128 a[44:47], v229 offset:6144\n"
    "ds_read_b128 a[48:51], v229 offset:8192\n"
    "ds_read_b128 a[52:55], v229 offset:10240\n"
    "ds_read_b128 a[56:59], v229 offset:12288\n"
    "ds_read_b128 a[60:63], v229 offset:14336\n"
    "s_waitcnt lgkmcnt(3)\n"
    "ds_write_b128 v235, a[48:51] offset:0\n"
    "s_waitcnt lgkmcnt(3)\n"
    "ds_write_b128 v235, a[52:55] offset:1024\n"
    "s_waitcnt lgkmcnt(3)\n"
    "ds_write_b128 v235, a[56:59] offset:2048\n"
    "s_waitcnt lgkmcnt(3)\n"
    "ds_write_b128 v235, a[60:63] offset:3072\n"
    "L_JB1_%=:\n"
    "v_perm_b32 v252, v238, v236, s34\n"
    "v_perm_b32 v253, v242, v240, s34\n"
    "v_perm_b32 v254, v246, v244, s34\n"
    "v_perm_b32 v255, v250, v248, s34\n"
    "buffer_store_dwordx4 v[252:255], v224, s[20:23], %14 offen offset:0 sc1\n"
    "s_nop 1\n"
    "v_perm_b32 v252, v238, v236, s35\n"
    "v_perm_b32 v253, v242, v240, s35\n"
    "v_perm_b32 v254, v246, v244, s35\n"
    "v_perm_b32 v255, v250, v248, s35\n"
    "buffer_store_dwordx4 v[252:255], v224, s[20:23], %14 offen offset:2048 sc1\n"
    "s_nop 1\n"
    "s_branch L_MB_%=\n"
    "L_HK4_%=:\n"
    "v_perm_b32 v252, v239, v237, s34\n"
    "v_perm_b32 v253, v243, v241, s34\n"
    "v_perm_b32 v254, v247, v245, s34\n"
    "v_perm_b32 v255, v251, v249, s34\n"
    "buffer_store_dwordx4 v[252:255], v224, s[20:23], s82 offen offset:0 sc1\n"
    "s_nop 1\n"
    "v_perm_b32 v252, v239, v237, s35\n"
    "v_perm_b32 v253, v243, v241, s35\n"
    "v_perm_b32 v254, v247, v245, s35\n"
    "v_perm_b32 v255, v251, v249, s35\n"
    "buffer_store_dwordx4 v[252:255], v224, s[20:23], s82 offen offset:2048 sc1\n"
    "s_nop 1\n"
    "s_branch L_MB_%=\n"
    "L_HK3_%=:\n"
    "s_cmp_lg_u32 s69, 3\n"
    "s_cbranch_scc1 L_G2P_%=\n"
    "v_add_u32 v228, s72, v230\n"
    "v_xor_b32 v229, 16, v228\n"
    "ds_read_b128 a[0:3], v228 offset:0\n"
    "ds_read_b128 a[4:7], v228 offset:2048\n"
    "ds_read_b128 a[8:11], v228 offset:4096\n"
    "ds_read_b128 a[12:15], v228 offset:6144\n"
    "ds_read_b128 a[16:19], v228 offset:8192\n"
    "ds_read_b128 a[20:23], v228 offset:10240\n"
    "ds_read_b128 a[24:27], v228 offset:12288\n"
    "ds_read_b128 a[28:31], v228 offset:14336\n"
    "ds_read_b128 a[32:35], v229 offset:0\n"
    "ds_read_b128 a[36:39], v229 offset:2048\n"
    "ds_read_b128 a[40:43], v229 offset:4096\n"
    "ds_read_b128 a[44:47], v229 offset:6144\n"
    "ds_read_b128 a[48:51], v229 offset:8192\n"
    "ds_read_b128 a[52:55], v229 offset:10240\n"
    "ds_read_b128 a[56:59], v229 offset:12288\n"
    "ds_read_b128 a[60:63], v229 offset:14336\n"
    "s_waitcnt lgkmcnt(3)\n"
    "ds_write_b128 v235, a[48:51] offset:0\n"
    "s_waitcnt lgkmcnt(3)\n"
    "ds_write_b128 v235, a[52:55] offset:1024\n"
    "s_waitcnt lgkmcnt(3)\n"
    "ds_write_b128 v235, a[56:59] offset:2048\n"
    "s_waitcnt lgkmcnt(3)\n"
    "ds_write_b128 v235, a[60:63] offset:3072\n"
    "s_cmp_lg_u32 %11, 3\n"
    "s_cbranch_scc1 L_MB_%=\n"
    "L_G2P_%=:\n"
    "v_mov_b32 v252, 1\n"
    "buffer_store_dword v252, v224, s[28:31], 0 offen offset:0 sc1\n"
    "s_branch L_MB_%=\n"
    "L_HK2_%=:\n"
    "s_mov_b32 s83, 16384\n"
    "buffer_load_dwordx4 v[252:255], v224, s[24:27], s83 offen sc1\n"
    "s_branch L_MB_%=\n"
    "L_HK1_%=:\n"
    "v_and_b32 v252, v252, v253\n"
    "v_and_b32 v254, v254, v255\n"
    "v_and_b32 v252, v252, v254\n"
    "v_cmp_eq_u32 vcc, 1, v252\n"
    "s_cmp_eq_u64 vcc, -1\n"
    "s_cbranch_scc1 L_MB_%=\n"
    "s_mov_b32 s75, 0x40000\n"
    "L_G2_%=:\n"
    "s_sleep 1\n"
    "s_mov_b32 s83, 16384\n"
    "buffer_load_dwordx4 v[252:255], v224, s[24:27], s83 offen sc1\n"
    "s_waitcnt vmcnt(0)\n"
    "v_and_b32 v252, v252, v253\n"
    "v_and_b32 v254, v254, v255\n"
    "v_and_b32 v252, v252, v254\n"
    "v_cmp_eq_u32 vcc, 1, v252\n"
    "s_cmp_eq_u64 vcc, -1\n"
    "s_cbranch_scc1 L_MB_%=\n"
    "s_sub_u32 s75, s75, 1\n"
    "s_cmp_lg_u32 s75, 0\n"
    "s_cbranch_scc1 L_G2_%=\n"
    "s_branch L_MB_%=\n"
    "L_S1_%=:\n"
    "s_waitcnt vmcnt(21) lgkmcnt(8)\n"
    "v_mfma_f32_16x16x32_bf16 v[0:3], a[96:99], a[160:163], v[0:3]\n"
    "buffer_load_dwordx4 a[152:155], v224, s[36:39], s60 offen offset:2048 sc1\n"
    "v_mfma_f32_16x16x32_bf16 v[0:3], a[100:103], a[164:167], v[0:3]\n"
    "buffer_load_dwordx4 a[156:159], v224, s[36:39], s60 offen offset:3072 sc1\n"
    "v_mfma_f32_16x16x32_bf16 v[4:7], a[100:103], a[172:175], v[4:7]\n"
    "s_add_u32 s60, s60, 4352\n"
    "v_add_u32 v228, s71, v230\n"
    "v_mfma_f32_16x16x32_bf16 v[4:7], a[96:99], a[168:171], v[4:7]\n"
    "v_xor_b32 v229, 16, v228\n"
    "v_add_u32 v232, s71, v224\n"
    "s_add_u32 s80, s80, 1\n"
    "v_mfma_f32_16x16x32_bf16 v[8:11], a[96:99], a[176:179], v[8:11]\n"
    "v_accvgpr_read_b32 v244, a48\n"
    "v_accvgpr_read_b32 v246, a49\n"
    "v_mfma_f32_16x16x32_bf16 v[8:11], a[100:103], a[180:183], v[8:11]\n"
    "v_accvgpr_read_b32 v237, a240\n"
    "v_lshlrev_b32 v245, 16, v244\n"
    "v_mfma_f32_16x16x32_bf16 v[12:15], a[100:103], a[188:191], v[12:15]\n"
    "v_lshlrev_b32 v247, 16, v246\n"
    "v_and_b32 v244, s73, v244\n"
    "v_mfma_f32_16x16x32_bf16 v[12:15], a[96:99], a[184:187], v[12:15]\n"
    "v_and_b32 v246, s73, v246\n"
    "v_mul_f32 v245, v245, v112\n"
    "s_waitcnt lgkmcnt(0)\n"
    "v_mfma_f32_16x16x32_bf16 v[16:19], a[96:99], a[192:195], v[16:19]\n"
    "v_mul_f32 v247, v247, v176\n"
    "v_mul_f32 v244, v244, v144\n"
    "v_mfma_f32_16x16x32_bf16 v[16:19], a[100:103], a[196:199], v[16:19]\n"
    "v_mul_f32 v246, v246, v237\n"
    "v_cvt_pk_bf16_f32 v236, v245, v244\n"
    "v_mfma_f32_16x16x32_bf16 v[20:23], a[100:103], a[204:207], v[20:23]\n"
    "v_cvt_pk_bf16_f32 v237, v247, v246\n"
    "v_accvgpr_read_b32 v244, a50\n"
    "v_mfma_f32_16x16x32_bf16 v[20:23], a[96:99], a[200:203], v[20:23]\n"
    "v_accvgpr_read_b32 v246, a51\n"
    "v_accvgpr_read_b32 v239, a241\n"
    "v_lshlrev_b32 v245, 16, v244\n"
    "v_mfma_f32_16x16x32_bf16 v[24:27], a[96:99], a[208:211], v[24:27]\n"
    "v_lshlrev_b32 v247, 16, v246\n"
    "v_and_b32 v244, s73, v244\n"
    "v_mfma_f32_16x16x32_bf16 v[24:27], a[100:103], a[212:215], v[24:27]\n"
    "v_and_b32 v246, s73, v246\n"
    "v_mul_f32 v245, v245, v113\n"
    "v_mfma_f32_16x16x32_bf16 v[28:31], a[100:103], a[220:223], v[28:31]\n"
    "v_mul_f32 v247, v247, v177\n"
    "v_mul_f32 v244, v244, v145\n"
    "v_mfma_f32_16x16x32_bf16 v[28:31], a[96:99], a[216:219], v[28:31]\n"
    "v_mul_f32 v246, v246, v239\n"
    "v_cvt_pk_bf16_f32 v238, v245, v244\n"
    "s_waitcnt vmcnt(20)\n"
    "v_mfma_f32_16x16x32_bf16 v[32:35], a[104:107], a[160:163], v[32:35]\n"
    "buffer_load_dwordx4 a[96:99], v224, s[36:39], s60 offen offset:0 sc1\n"
    "v_mfma_f32_16x16x32_bf16 v[32:35], a[108:111], a[164:167], v[32:35]\n"
    "buffer_load_dwordx4 a[100:103], v224, s[36:39], s60 offen offset:1024 sc1\n"
    "v_mfma_f32_16x16x32_bf16 v[36:39], a[108:111], a[172:175], v[36:39]\n"
    "v_cvt_pk_bf16_f32 v239, v247, v246\n"
    "ds_write_b128 v228, v[236:239] offset:0\n"
    "v_mfma_f32_16x16x32_bf16 v[36:39], a[104:107], a[168:171], v[36:39]\n"
    "v_accvgpr_read_b32 v244, a80\n"
    "v_accvgpr_read_b32 v246, a81\n"
    "v_accvgpr_read_b32 v237, a242\n"
    "v_mfma_f32_16x16x32_bf16 v[40:43], a[104:107], a[176:179], v[40:43]\n"
    "v_lshlrev_b32 v245, 16, v244\n"
    "v_lshlrev_b32 v247, 16, v246\n"
    "v_mfma_f32_16x16x32_bf16 v[40:43], a[108:111], a[180:183], v[40:43]\n"
    "v_and_b32 v244, s73, v244\n"
    "v_and_b32 v246, s73, v246\n"
    "v_mfma_f32_16x16x32_bf16 v[44:47], a[108:111], a[188:191], v[44:47]\n"
    "v_mul_f32 v245, v245, v114\n"
    "v_mul_f32 v247, v247, v178\n"
    "v_mul_f32 v244, v244, v146\n"
    "v_mfma_f32_16x16x32_bf16 v[44:47], a[104:107], a[184:187], v[44:47]\n"
    "v_mul_f32 v246, v246, v237\n"
    "v_cvt_pk_bf16_f32 v236, v245, v244\n"
    "v_mfma_f32_16x16x32_bf16 v[48:51], a[104:107], a[192:195], v[48:51]\n"
    "v_cvt_pk_bf16_f32 v237, v247, v246\n"
    "v_accvgpr_read_b32 v244, a82\n"
    "v_mfma_f32_16x16x32_bf16 v[48:51], a[108:111], a[196:199], v[48:51]\n"
    "v_accvgpr_read_b32 v246, a83\n"
    "v_accvgpr_read_b32 v239, a243\n"
    "v_mfma_f32_16x16x32_bf16 v[52:55], a[108:111], a[204:207], v[52:55]\n"
    "v_lshlrev_b32 v245, 16, v244\n"
    "v_lshlrev_b32 v247, 16, v246\n"
    "v_and_b32 v244, s73, v244\n"
    "v_mfma_f32_16x16x32_bf16 v[52:55], a[104:107], a[200:203], v[52:55]\n"
    "v_and_b32 v246, s73, v246\n"
    "v_mul_f32 v245, v245, v115\n"
    "v_mfma_f32_16x16x32_bf16 v[56:59], a[104:107], a[208:211], v[56:59]\n"
    "v_mul_f32 v247, v247, v179\n"
    "v_mul_f32 v244, v244, v147\n"
    "v_mfma_f32_16x16x32_bf16 v[56:59], a[108:111], a[212:215], v[56:59]\n"
    "v_mul_f32 v246, v246, v239\n"
    "v_cvt_pk_bf16_f32 v238, v245, v244\n"
    "v_mfma_f32_16x16x32_bf16 v[60:63], a[108:111], a[220:223], v[60:63]\n"
    "v_cvt_pk_bf16_f32 v239, v247, v246\n"
    "ds_write_b128 v229, v[236:239] offset:0\n"
    "v_mfma_f32_16x16x32_bf16 v[60:63], a[104:107], a[216:219], v[60:63]\n"
    "v_accvgpr_read_b32 v244, a52\n"
    "s_waitcnt vmcnt(18)\n"
    "v_mfma_f32_16x16x32_bf16 v[64:67], a[112:115], a[160:163], v[64:67]\n"
    "buffer_load_dwordx4 a[104:107], v224, s[36:39], s60 offen offset:2048 sc1\n"
    "v_mfma_f32_16x16x32_bf16 v[64:67], a[116:119], a[164:167], v[64:67]\n"
    "buffer_load_dwordx4 a[108:111], v224, s[36:39], s60 offen offset:3072 sc1\n"
    "v_mfma_f32_16x16x32_bf16 v[68:71], a[116:119], a[172:175], v[68:71]\n"
    "s_add_u32 s60, s60, 4352\n"
    "v_accvgpr_read_b32 v246, a53\n"
    "v_accvgpr_read_b32 v237, a244\n"
    "v_mfma_f32_16x16x32_bf16 v[68:71], a[112:115], a[168:171], v[68:71]\n"
    "v_lshlrev_b32 v245, 16, v244\n"
    "v_lshlrev_b32 v247, 16, v246\n"
    "v_mfma_f32_16x16x32_bf16 v[72:75], a[112:115], a[176:179], v[72:75]\n"
    "v_and_b32 v244, s73, v244\n"
    "v_and_b32 v246, s73, v246\n"
    "v_mfma_f32_16x16x32_bf16 v[72:75], a[116:119], a[180:183], v[72:75]\n"
    "v_mul_f32 v245, v245, v116\n"
    "v_mul_f32 v247, v247, v180\n"
    "v_mul_f32 v244, v244, v148\n"
    "v_mfma_f32_16x16x32_bf16 v[76:79], a[116:119], a[188:191], v[76:79]\n"
    "v_mul_f32 v246, v246, v237\n"
    "v_cvt_pk_bf16_f32 v236, v245, v244\n"
    "v_mfma_f32_16x16x32_bf16 v[76:79], a[112:115], a[184:187], v[76:79]\n"
    "v_cvt_pk_bf16_f32 v237, v247, v246\n"
    "v_accvgpr_read_b32 v244, a54\n"
    "v_mfma_f32_16x16x32_bf16 v[80:83], a[112:115], a[192:195], v[80:83]\n"
    "v_accvgpr_read_b32 v246, a55\n"
    "v_accvgpr_read_b32 v239, a245\n"
    "v_mfma_f32_16x16x32_bf16 v[80:83], a[116:119], a[196:199], v[80:83]\n"
    "v_lshlrev_b32 v245, 16, v244\n"
    "v_lshlrev_b32 v247, 16, v246\n"
    "v_and_b32 v244, s73, v244\n"
    "v_mfma_f32_16x16x32_bf16 v[84:87], a[116:119], a[204:207], v[84:87]\n"
    "v_and_b32 v246, s73, v246\n"
    "v_mul_f32 v245, v245, v117\n"
    "v_mfma_f32_16x16x32_bf16 v[84:87], a[112:115], a[200:203], v[84:87]\n"
    "v_mul_f32 v247, v247, v181\n"
    "v_mul_f32 v244, v244, v149\n"
    "v_mfma_f32_16x16x32_bf16 v[88:91], a[112:115], a[208:211], v[88:91]\n"
    "v_mul_f32 v246, v246, v239\n"
    "v_cvt_pk_bf16_f32 v238, v245, v244\n"
    "v_mfma_f32_16x16x32_bf16 v[88:91], a[116:119], a[212:215], v[88:91]\n"
    "v_cvt_pk_bf16_f32 v239, v247, v246\n"
    "ds_write_b128 v228, v[236:239] offset:2048\n"
    "v_mfma_f32_16x16x32_bf16 v[92:95], a[116:119], a[220:223], v[92:95]\n"
    "v_accvgpr_read_b32 v244, a84\n"
    "v_accvgpr_read_b32 v246, a85\n"
    "v_mfma_f32_16x16x32_bf16 v[92:95], a[112:115], a[216:219], v[92:95]\n"
    "buffer_load_dwordx4 a[112:115], v224, s[36:39], s60 offen offset:0 sc1\n"
    "s_waitcnt vmcnt(19)\n"
    "v_mfma_f32_16x16x32_bf16 a[224:227], a[120:123], a[160:163], a[224:227]\n"
    "ds_read_b128 a[160:163], v226 offset:16384\n"
    "buffer_load_dwordx4 a[116:119], v224, s[36:39], s60 offen offset:1024 sc1\n"
    "v_mfma_f32_16x16x32_bf16 a[224:227], a[124:127], a[164:167], a[224:227]\n"
    "ds_read_b128 a[164:167], v227 offset:16384\n"
    "v_accvgpr_read_b32 v237, a246\n"
    "v_lshlrev_b32 v245, 16, v244\n"
    "v_mfma_f32_16x16x32_bf16 a[228:231], a[124:127], a[172:175], a[228:231]\n"
    "ds_read_b128 a[172:175], v227 offset:18432\n"
    "v_lshlrev_b32 v247, 16, v246\n"
    "v_mfma_f32_16x16x32_bf16 a[228:231], a[120:123], a[168:171], a[228:231]\n"
    "ds_read_b128 a[168:171], v226 offset:18432\n"
    "v_and_b32 v244, s73, v244\n"
    "v_mfma_f32_16x16x32_bf16 a[232:235], a[120:123], a[176:179], a[232:235]\n"
    "ds_read_b128 a[176:179], v226 offset:20480\n"
    "v_and_b32 v246, s73, v246\n"
    "v_mul_f32 v245, v245, v118\n"
    "v_mfma_f32_16x16x32_bf16 a[232:235], a[124:127], a[180:183], a[232:235]\n"
    "ds_read_b128 a[180:183], v227 offset:20480\n"
    "v_mul_f32 v247, v247, v182\n"
    "v_mfma_f32_16x16x32_bf16 a[236:239], a[124:127], a[188:191], a[236:239]\n"
    "ds_read_b128 a[188:191], v227 offset:22528\n"
    "v_mul_f32 v244, v244, v150\n"
    "v_mfma_f32_16x16x32_bf16 a[236:239], a[120:123], a[184:187], a[236:239]\n"
    "ds_read_b128 a[184:187], v226 offset:22528\n"
    "v_mul_f32 v246, v246, v237\n"
    "v_mfma_f32_16x16x32_bf16 v[96:99], a[120:123], a[192:195], v[96:99]\n"
    "ds_read_b128 a[192:195], v226 offset:24576\n"
    "v_cvt_pk_bf16_f32 v236, v245, v244\n"
    "v_cvt_pk_bf16_f32 v237, v247, v246\n"
    "v_mfma_f32_16x16x32_bf16 v[96:99], a[124:127], a[196:199], v[96:99]\n"
    "ds_read_b128 a[196:199], v227 offset:24576\n"
    "v_accvgpr_read_b32 v244, a86\n"
    "v_mfma_f32_16x16x32_bf16 v[100:103], a[124:127], a[204:207], v[100:103]\n"
    "ds_read_b128 a[204:207], v227 offset:26624\n"
    "v_accvgpr_read_b32 v246, a87\n"
    "v_mfma_f32_16x16x32_bf16 v[100:103], a[120:123], a[200:203], v[100:103]\n"
    "ds_read_b128 a[200:203], v226 offset:26624\n"
    "v_accvgpr_read_b32 v239, a247\n"
    "v_mfma_f32_16x16x32_bf16 v[104:107], a[120:123], a[208:211], v[104:107]\n"
    "ds_read_b128 a[208:211], v226 offset:28672\n"
    "v_lshlrev_b32 v245, 16, v244\n"
    "v_lshlrev_b32 v247, 16, v246\n"
    "v_mfma_f32_16x16x32_bf16 v[104:107], a[124:127], a[212:215], v[104:107]\n"
    "ds_read_b128 a[212:215], v227 offset:28672\n"
    "v_and_b32 v244, s73, v244\n"
    "v_mfma_f32_16x16x32_bf16 v[108:111], a[124:127], a[220:223], v[108:111]\n"
    "ds_read_b128 a[220:223], v227 offset:30720\n"
    "v_and_b32 v246, s73, v246\n"
    "v_mfma_f32_16x16x32_bf16 v[108:111], a[120:123], a[216:219], v[108:111]\n"
    "ds_read_b128 a[216:219], v226 offset:30720\n"
    "s_waitcnt vmcnt(16) lgkmcnt(8)\n"
    "v_mfma_f32_16x16x32_bf16 v[0:3], a[128:131], a[160:163], v[0:3]\n"
    "buffer_load_dwordx4 a[120:123], v224, s[36:39], s60 offen offset:2048 sc1\n"
    "v_mul_f32 v245, v245, v119\n"
    "v_mfma_f32_16x16x32_bf16 v[0:3], a[132:135], a[164:167], v[0:3]\n"
    "buffer_load_dwordx4 a[124:127], v224, s[36:39], s60 offen offset:3072 sc1\n"
    "v_mfma_f32_16x16x32_bf16 v[4:7], a[132:135], a[172:175], v[4:7]\n"
    "s_add_u32 s60, s60, 4352\n"
    "v_mul_f32 v247, v247, v183\n"
    "v_mfma_f32_16x16x32_bf16 v[4:7], a[128:131], a[168:171], v[4:7]\n"
    "v_mul_f32 v244, v244, v151\n"
    "v_mul_f32 v246, v246, v239\n"
    "v_cvt_pk_bf16_f32 v238, v245, v244\n"
    "v_mfma_f32_16x16x32_bf16 v[8:11], a[128:131], a[176:179], v[8:11]\n"
    "v_cvt_pk_bf16_f32 v239, v247, v246\n"
    "ds_write_b128 v229, v[236:239] offset:2048\n"
    "v_mfma_f32_16x16x32_bf16 v[8:11], a[132:135], a[180:183], v[8:11]\n"
    "v_accvgpr_read_b32 v244, a56\n"
    "v_accvgpr_read_b32 v246, a57\n"
    "v_mfma_f32_16x16x32_bf16 v[12:15], a[132:135], a[188:191], v[12:15]\n"
    "v_accvgpr_read_b32 v237, a248\n"
    "v_lshlrev_b32 v245, 16, v244\n"
    "v_mfma_f32_16x16x32_bf16 v[12:15], a[128:131], a[184:187], v[12:15]\n"
    "v_lshlrev_b32 v247, 16, v246\n"
    "v_and_b32 v244, s73, v244\n"
    "s_waitcnt lgkmcnt(1)\n"
    "v_mfma_f32_16x16x32_bf16 v[16:19], a[128:131], a[192:195], v[16:19]\n"
    "v_and_b32 v246, s73, v246\n"
    "v_mul_f32 v245, v245, v120\n"
    "v_mfma_f32_16x16x32_bf16 v[16:19], a[132:135], a[196:199], v[16:19]\n"
    "v_mul_f32 v247, v247, v184\n"
    "v_mul_f32 v244, v244, v152\n"
    "v_mfma_f32_16x16x32_bf16 v[20:23], a[132:135], a[204:207], v[20:23]\n"
    "v_mul_f32 v246, v246, v237\n"
    "v_cvt_pk_bf16_f32 v236, v245, v244\n"
    "v_mfma_f32_16x16x32_bf16 v[20:23], a[128:131], a[200:203], v[20:23]\n"
    "v_cvt_pk_bf16_f32 v237, v247, v246\n"
    "v_accvgpr_read_b32 v244, a58\n"
    "v_accvgpr_read_b32 v246, a59\n"
    "v_mfma_f32_16x16x32_bf16 v[24:27], a[128:131], a[208:211], v[24:27]\n"
    "v_accvgpr_read_b32 v239, a249\n"
    "v_lshlrev_b32 v245, 16, v244\n"
    "v_mfma_f32_16x16x32_bf16 v[24:27], a[132:135], a[212:215], v[24:27]\n"
    "v_lshlrev_b32 v247, 16, v246\n"
    "v_and_b32 v244, s73, v244\n"
    "v_mfma_f32_16x16x32_bf16 v[28:31], a[132:135], a[220:223], v[28:31]\n"
    "v_and_b32 v246, s73, v246\n"
    "v_mul_f32 v245, v245, v121\n"
    "v_mfma_f32_16x16x32_bf16 v[28:31], a[128:131], a[216:219], v[28:31]\n"
    "v_mul_f32 v247, v247, v185\n"
    "v_mul_f32 v244, v244, v153\n"
    "s_waitcnt vmcnt(15)\n"
    "v_mfma_f32_16x16x32_bf16 v[32:35], a[136:139], a[160:163], v[32:35]\n"
    "buffer_load_dwordx4 a[128:131], v224, s[36:39], s60 offen offset:0 sc1\n"
    "v_mfma_f32_16x16x32_bf16 v[32:35], a[140:143], a[164:167], v[32:35]\n"
    "buffer_load_dwordx4 a[132:135], v224, s[36:39], s60 offen offset:1024 sc1\n"
    "v_mfma_f32_16x16x32_bf16 v[36:39], a[140:143], a[172:175], v[36:39]\n"
    "v_mul_f32 v246, v246, v239\n"
    "v_cvt_pk_bf16_f32 v238, v245, v244\n"
    "v_mfma_f32_16x16x32_bf16 v[36:39], a[136:139], a[168:171], v[36:39]\n"
    "v_cvt_pk_bf16_f32 v239, v247, v246\n"
    "ds_write_b128 v228, v[236:239] offset:4096\n"
    "v_mfma_f32_16x16x32_bf16 v[40:43], a[136:139], a[176:179], v[40:43]\n"
    "v_accvgpr_read_b32 v244, a88\n"
    "v_accvgpr_read_b32 v246, a89\n"
    "v_mfma_f32_16x16x32_bf16 v[40:43], a[140:143], a[180:183], v[40:43]\n"
    "v_accvgpr_read_b32 v237, a250\n"
    "v_lshlrev_b32 v245, 16, v244\n"
    "v_mfma_f32_16x16x32_bf16 v[44:47], a[140:143], a[188:191], v[44:47]\n"
    "v_lshlrev_b32 v247, 16, v246\n"
    "v_and_b32 v244, s73, v244\n"
    "v_and_b32 v246, s73, v246\n"
    "v_mfma_f32_16x16x32_bf16 v[44:47], a[136:139], a[184:187], v[44:47]\n"
    "v_mul_f32 v245, v245, v122\n"
    "v_mul_f32 v247, v247, v186\n"
    "v_mfma_f32_16x16x32_bf16 v[48:51], a[136:139], a[192:195], v[48:51]\n"
    "v_mul_f32 v244, v244, v154\n"
    "v_mul_f32 v246, v246, v237\n"
    "v_mfma_f32_16x16x32_bf16 v[48:51], a[140:143], a[196:199], v[48:51]\n"
    "v_cvt_pk_bf16_f32 v236, v245, v244\n"
    "v_cvt_pk_bf16_f32 v237, v247, v246\n"
    "v_mfma_f32_16x16x32_bf16 v[52:55], a[140:143], a[204:207], v[52:55]\n"
    "v_accvgpr_read_b32 v244, a90\n"
    "v_accvgpr_read_b32 v246, a91\n"
    "v_accvgpr_read_b32 v239, a251\n"
    "v_mfma_f32_16x16x32_bf16 v[52:55], a[136:139], a[200:203], v[52:55]\n"
    "v_lshlrev_b32 v245, 16, v244\n"
    "v_lshlrev_b32 v247, 16, v246\n"
    "v_mfma_f32_16x16x32_bf16 v[56:59], a[136:139], a[208:211], v[56:59]\n"
    "v_and_b32 v244, s73, v244\n"
    "v_and_b32 v246, s73, v246\n"
    "v_mfma_f32_16x16x32_bf16 v[56:59], a[140:143], a[212:215], v[56:59]\n"
    "v_mul_f32 v245, v245, v123\n"
    "v_mul_f32 v247, v247, v187\n"
    "v_mfma_f32_16x16x32_bf16 v[60:63], a[140:143], a[220:223], v[60:63]\n"
    "v_mul_f32 v244, v244, v155\n"
    "v_mul_f32 v246, v246, v239\n"
    "v_cvt_pk_bf16_f32 v238, v245, v244\n"
    "v_mfma_f32_16x16x32_bf16 v[60:63], a[136:139], a[216:219], v[60:63]\n"
    "v_cvt_pk_bf16_f32 v239, v247, v246\n"
    "s_waitcnt vmcnt(14)\n"
    "v_mfma_f32_16x16x32_bf16 v[64:67], a[144:147], a[160:163], v[64:67]\n"
    "buffer_load_dwordx4 a[136:139], v224, s[36:39], s60 offen offset:2048 sc1\n"
    "v_mfma_f32_16x16x32_bf16 v[64:67], a[148:151], a[164:167], v[64:67]\n"
    "buffer_load_dwordx4 a[140:143], v224, s[36:39], s60 offen offset:3072 sc1\n"
    "v_mfma_f32_16x16x32_bf16 v[68:71], a[148:151], a[172:175], v[68:71]\n"
    "s_add_u32 s60, s60, 4352\n"
    "ds_write_b128 v229, v[236:239] offset:4096\n"
    "v_mfma_f32_16x16x32_bf16 v[68:71], a[144:147], a[168:171], v[68:71]\n"
    "v_accvgpr_read_b32 v244, a60\n"
    "v_accvgpr_read_b32 v246, a61\n"
    "v_mfma_f32_16x16x32_bf16 v[72:75], a[144:147], a[176:179], v[72:75]\n"
    "v_accvgpr_read_b32 v237, a252\n"
    "v_lshlrev_b32 v245, 16, v244\n"
    "v_mov_b32 v233, s80\n"
    "s_waitcnt vmcnt(14)\n"
    "buffer_store_dword v233, v224, s[48:51], 0 offen sc1\n"
    "v_mfma_f32_16x16x32_bf16 v[72:75], a[148:151], a[180:183], v[72:75]\n"
    "v_lshlrev_b32 v247, 16, v246\n"
    "v_and_b32 v244, s73, v244\n"
    "v_and_b32 v246, s73, v246\n"
    "v_mfma_f32_16x16x32_bf16 v[76:79], a[148:151], a[188:191], v[76:79]\n"
    "v_mul_f32 v245, v245, v124\n"
    "v_mul_f32 v247, v247, v188\n"
    "v_mfma_f32_16x16x32_bf16 v[76:79], a[144:147], a[184:187], v[76:79]\n"
    "v_mul_f32 v244, v244, v156\n"
    "v_mul_f32 v246, v246, v237\n"
    "v_mfma_f32_16x16x32_bf16 v[80:83], a[144:147], a[192:195], v[80:83]\n"
    "v_cvt_pk_bf16_f32 v236, v245, v244\n"
    "v_cvt_pk_bf16_f32 v237, v247, v246\n"
    "v_mfma_f32_16x16x32_bf16 v[80:83], a[148:151], a[196:199], v[80:83]\n"
    "v_accvgpr_read_b32 v244, a62\n"
    "v_accvgpr_read_b32 v246, a63\n"
    "v_accvgpr_read_b32 v239, a253\n"
    "v_mfma_f32_16x16x32_bf16 v[84:87], a[148:151], a[204:207], v[84:87]\n"
    "v_lshlrev_b32 v245, 16, v244\n"
    "v_lshlrev_b32 v247, 16, v246\n"
    "v_mfma_f32_16x16x32_bf16 v[84:87], a[144:147], a[200:203], v[84:87]\n"
    "v_and_b32 v244, s73, v244\n"
    "v_and_b32 v246, s73, v246\n"
    "v_mfma_f32_16x16x32_bf16 v[88:91], a[144:147], a[208:211], v[88:91]\n"
    "v_mul_f32 v245, v245, v125\n"
    "v_mul_f32 v247, v247, v189\n"
    "v_mfma_f32_16x16x32_bf16 v[88:91], a[148:151], a[212:215], v[88:91]\n"
    "v_mul_f32 v244, v244, v157\n"
    "v_mul_f32 v246, v246, v239\n"
    "v_cvt_pk_bf16_f32 v238, v245, v244\n"
    "v_mfma_f32_16x16x32_bf16 v[92:95], a[148:151], a[220:223], v[92:95]\n"
    "v_cvt_pk_bf16_f32 v239, v247, v246\n"
    "ds_write_b128 v228, v[236:239] offset:6144\n"
    "v_mfma_f32_16x16x32_bf16 v[92:95], a[144:147], a[216:219], v[92:95]\n"
    "buffer_load_dwordx4 a[144:147], v224, s[36:39], s60 offen offset:0 sc1\n"
    "s_waitcnt vmcnt(14)\n"
    "v_mfma_f32_16x16x32_bf16 a[224:227], a[152:155], a[160:163], a[224:227]\n"
    "ds_read_b128 a[160:163], v226 offset:32768\n"
    "buffer_load_dwordx4 a[148:151], v224, s[36:39], s60 offen offset:1024 sc1\n"
    "v_mfma_f32_16x16x32_bf16 a[224:227], a[156:159], a[164:167], a[224:227]\n"
    "ds_read_b128 a[164:167], v227 offset:32768\n"
    "v_accvgpr_read_b32 v244, a92\n"
    "v_accvgpr_read_b32 v246, a93\n"
    "v_mfma_f32_16x16x32_bf16 a[228:231], a[156:159], a[172:175], a[228:231]\n"
    "ds_read_b128 a[172:175], v227 offset:34816\n"
    "v_accvgpr_read_b32 v237, a254\n"
    "v_mfma_f32_16x16x32_bf16 a[228:231], a[152:155], a[168:171], a[228:231]\n"
    "ds_read_b128 a[168:171], v226 offset:34816\n"
    "v_lshlrev_b32 v245, 16, v244\n"
    "v_mfma_f32_16x16x32_bf16 a[232:235], a[152:155], a[176:179], a[232:235]\n"
    "ds_read_b128 a[176:179], v226 offset:36864\n"
    "v_lshlrev_b32 v247, 16, v246\n"
    "v_and_b32 v244, s73, v244\n"
    "v_mfma_f32_16x16x32_bf16 a[232:235], a[156:159], a[180:183], a[232:235]\n"
    "ds_read_b128 a[180:183], v227 offset:36864\n"
    "v_and_b32 v246, s73, v246\n"
    "v_mfma_f32_16x16x32_bf16 a[236:239], a[156:159], a[188:191], a[236:239]\n"
    "ds_read_b128 a[188:191], v227 offset:38912\n"
    "v_mul_f32 v245, v245, v126\n"
    "v_mfma_f32_16x16x32_bf16 a[236:239], a[152:155], a[184:187], a[236:239]\n"
    "ds_read_b128 a[184:187], v226 offset:38912\n"
    "v_mul_f32 v247, v247, v190\n"
    "v_mfma_f32_16x16x32_bf16 v[96:99], a[152:155], a[192:195], v[96:99]\n"
    "ds_read_b128 a[192:195], v226 offset:40960\n"
    "v_mul_f32 v244, v244, v158\n"
    "v_mul_f32 v246, v246, v237\n"
    "v_mfma_f32_16x16x32_bf16 v[96:99], a[156:159], a[196:199], v[96:99]\n"
    "ds_read_b128 a[196:199], v227 offset:40960\n"
    "v_cvt_pk_bf16_f32 v236, v245, v244\n"
    "v_mfma_f32_16x16x32_bf16 v[100:103], a[156:159], a[204:207], v[100:103]\n"
    "ds_read_b128 a[204:207], v227 offset:43008\n"
    "v_cvt_pk_bf16_f32 v237, v247, v246\n"
    "v_mfma_f32_16x16x32_bf16 v[100:103], a[152:155], a[200:203], v[100:103]\n"
    "ds_read_b128 a[200:203], v226 offset:43008\n"
    "v_accvgpr_read_b32 v244, a94\n"
    "v_mfma_f32_16x16x32_bf16 v[104:107], a[152:155], a[208:211], v[104:107]\n"
    "ds_read_b128 a[208:211], v226 offset:45056\n"
    "v_accvgpr_read_b32 v246, a95\n"
    "v_accvgpr_read_b32 v239, a255\n"
    "v_mfma_f32_16x16x32_bf16 v[104:107], a[156:159], a[212:215], v[104:107]\n"
    "ds_read_b128 a[212:215], v227 offset:45056\n"
    "v_lshlrev_b32 v245, 16, v244\n"
    "v_mfma_f32_16x16x32_bf16 v[108:111], a[156:159], a[220:223], v[108:111]\n"
    "ds_read_b128 a[220:223], v227 offset:47104\n"
    "v_lshlrev_b32 v247, 16, v246\n"
    "v_mfma_f32_16x16x32_bf16 v[108:111], a[152:155], a[216:219], v[108:111]\n"
    "ds_read_b128 a[216:219], v226 offset:47104\n"
    "s_waitcnt vmcnt(13) lgkmcnt(8)\n"
    "v_mfma_f32_16x16x32_bf16 v[0:3], a[96:99], a[160:163], v[0:3]\n"
    "buffer_load_dwordx4 a[152:155], v224, s[36:39], s60 offen offset:2048 sc1\n"
    "v_and_b32 v244, s73, v244\n"
    "v_mfma_f32_16x16x32_bf16 v[0:3], a[100:103], a[164:167], v[0:3]\n"
    "buffer_load_dwordx4 a[156:159], v224, s[36:39], s60 offen offset:3072 sc1\n"
    "v_mfma_f32_16x16x32_bf16 v[4:7], a[100:103], a[172:175], v[4:7]\n"
    "s_add_u32 s60, s60, 4352\n"
    "v_and_b32 v246, s73, v246\n"
    "v_mfma_f32_16x16x32_bf16 v[4:7], a[96:99], a[168:171], v[4:7]\n"
    "ds_read_b128 v[240:243], v232 offset:0\n"
    "v_mul_f32 v245, v245, v127\n"
    "v_mul_f32 v247, v247, v191\n"
    "v_mfma_f32_16x16x32_bf16 v[8:11], a[96:99], a[176:179], v[8:11]\n"
    "v_mul_f32 v244, v244, v159\n"
    "v_mul_f32 v246, v246, v239\n"
    "v_mfma_f32_16x16x32_bf16 v[8:11], a[100:103], a[180:183], v[8:11]\n"
    "v_cvt_pk_bf16_f32 v238, v245, v244\n"
    "v_cvt_pk_bf16_f32 v239, v247, v246\n"
    "v_mfma_f32_16x16x32_bf16 v[12:15], a[100:103], a[188:191], v[12:15]\n"
    "ds_write_b128 v229, v[236:239] offset:6144\n"
    "v_mfma_f32_16x16x32_bf16 v[12:15], a[96:99], a[184:187], v[12:15]\n"
    "ds_read_b128 v[248:251], v235 offset:4096\n"
    "v_accvgpr_read_b32 v244, a64\n"
    "s_waitcnt lgkmcnt(3)\n"
    "v_mfma_f32_16x16x32_bf16 v[16:19], a[96:99], a[192:195], v[16:19]\n"
    "v_accvgpr_read_b32 v246, a65\n"
    "v_lshlrev_b32 v245, 16, v244\n"
    "v_mfma_f32_16x16x32_bf16 v[16:19], a[100:103], a[196:199], v[16:19]\n"
    "v_lshlrev_b32 v247, 16, v246\n"
    "v_and_b32 v244, s73, v244\n"
    "v_mfma_f32_16x16x32_bf16 v[20:23], a[100:103], a[204:207], v[20:23]\n"
    "v_and_b32 v246, s73, v246\n"
    "v_mul_f32 v245, v245, v128\n"
    "v_mfma_f32_16x16x32_bf16 v[20:23], a[96:99], a[200:203], v[20:23]\n"
    "v_mul_f32 v247, v247, v192\n"
    "v_mul_f32 v244, v244, v160\n"
    "v_mul_f32 v246, v246, v208\n"
    "v_mfma_f32_16x16x32_bf16 v[24:27], a[96:99], a[208:211], v[24:27]\n"
    "v_cvt_pk_bf16_f32 v236, v245, v244\n"
    "v_cvt_pk_bf16_f32 v237, v247, v246\n"
    "v_mfma_f32_16x16x32_bf16 v[24:27], a[100:103], a[212:215], v[24:27]\n"
    "s_waitcnt lgkmcnt(2)\n"
    "buffer_store_dwordx4 v[240:243], v231, s[44:47], s77 offen offset:128 sc1\n"
    "v_mfma_f32_16x16x32_bf16 v[28:31], a[100:103], a[220:223], v[28:31]\n"
    "v_accvgpr_read_b32 v244, a66\n"
    "v_accvgpr_read_b32 v246, a67\n"
    "v_mfma_f32_16x16x32_bf16 v[28:31], a[96:99], a[216:219], v[28:31]\n"
    "v_lshlrev_b32 v245, 16, v244\n"
    "v_lshlrev_b32 v247, 16, v246\n"
    "s_waitcnt vmcnt(14)\n"
    "v_mfma_f32_16x16x32_bf16 v[32:35], a[104:107], a[160:163], v[32:35]\n"
    "buffer_load_dwordx4 a[96:99], v224, s[36:39], s60 offen offset:0 sc1\n"
    "v_mfma_f32_16x16x32_bf16 v[32:35], a[108:111], a[164:167], v[32:35]\n"
    "buffer_load_dwordx4 a[100:103], v224, s[36:39], s60 offen offset:1024 sc1\n"
    "v_mfma_f32_16x16x32_bf16 v[36:39], a[108:111], a[172:175], v[36:39]\n"
    "v_and_b32 v244, s73, v244\n"
    "v_and_b32 v246, s73, v246\n"
    "v_mfma_f32_16x16x32_bf16 v[36:39], a[104:107], a[168:171], v[36:39]\n"
    "v_mul_f32 v245, v245, v129\n"
    "v_mul_f32 v247, v247, v193\n"
    "v_mul_f32 v244, v244, v161\n"
    "v_mfma_f32_16x16x32_bf16 v[40:43], a[104:107], a[176:179], v[40:43]\n"
    "v_mul_f32 v246, v246, v209\n"
    "v_cvt_pk_bf16_f32 v238, v245, v244\n"
    "v_mfma_f32_16x16x32_bf16 v[40:43], a[108:111], a[180:183], v[40:43]\n"
    "v_cvt_pk_bf16_f32 v239, v247, v246\n"
    "ds_write_b128 v228, v[236:239] offset:8192\n"
    "v_mfma_f32_16x16x32_bf16 v[44:47], a[108:111], a[188:191], v[44:47]\n"
    "ds_read_b128 v[252:255], v232 offset:1024\n"
    "s_waitcnt lgkmcnt(2)\n"
    "v_lshlrev_b32 v245, 16, v248\n"
    "v_lshlrev_b32 v247, 16, v249\n"
    "v_mfma_f32_16x16x32_bf16 v[44:47], a[104:107], a[184:187], v[44:47]\n"
    "v_and_b32 v244, s73, v248\n"
    "v_and_b32 v246, s73, v249\n"
    "v_mfma_f32_16x16x32_bf16 v[48:51], a[104:107], a[192:195], v[48:51]\n"
    "v_mul_f32 v245, v245, v130\n"
    "v_mul_f32 v247, v247, v194\n"
    "v_mfma_f32_16x16x32_bf16 v[48:51], a[108:111], a[196:199], v[48:51]\n"
    "v_mul_f32 v244, v244, v162\n"
    "v_mul_f32 v246, v246, v210\n"
    "v_mfma_f32_16x16x32_bf16 v[52:55], a[108:111], a[204:207], v[52:55]\n"
    "v_cvt_pk_bf16_f32 v236, v245, v244\n"
    "v_cvt_pk_bf16_f32 v237, v247, v246\n"
    "v_lshlrev_b32 v245, 16, v250\n"
    "v_mfma_f32_16x16x32_bf16 v[52:55], a[104:107], a[200:203], v[52:55]\n"
    "v_lshlrev_b32 v247, 16, v251\n"
    "v_and_b32 v244, s73, v250\n"
    "v_mfma_f32_16x16x32_bf16 v[56:59], a[104:107], a[208:211], v[56:59]\n"
    "v_and_b32 v246, s73, v251\n"
    "v_mul_f32 v245, v245, v131\n"
    "v_mfma_f32_16x16x32_bf16 v[56:59], a[108:111], a[212:215], v[56:59]\n"
    "v_mul_f32 v247, v247, v195\n"
    "v_mul_f32 v244, v244, v163\n"
    "v_mfma_f32_16x16x32_bf16 v[60:63], a[108:111], a[220:223], v[60:63]\n"
    "v_mul_f32 v246, v246, v211\n"
    "v_cvt_pk_bf16_f32 v238, v245, v244\n"
    "v_cvt_pk_bf16_f32 v239, v247, v246\n"
    "v_mfma_f32_16x16x32_bf16 v[60:63], a[104:107], a[216:219], v[60:63]\n"
    "ds_write_b128 v229, v[236:239] offset:8192\n"
    "s_waitcnt vmcnt(14)\n"
    "v_mfma_f32_16x16x32_bf16 v[64:67], a[112:115], a[160:163], v[64:67]\n"
    "buffer_load_dwordx4 a[104:107], v224, s[36:39], s60 offen offset:2048 sc1\n"
    "v_mfma_f32_16x16x32_bf16 v[64:67], a[116:119], a[164:167], v[64:67]\n"
    "buffer_load_dwordx4 a[108:111], v224, s[36:39], s60 offen offset:3072 sc1\n"
    "v_mfma_f32_16x16x32_bf16 v[68:71], a[116:119], a[172:175], v[68:71]\n"
    "s_add_u32 s60, s60, 4352\n"
    "ds_read_b128 v[248:251], v235 offset:5120\n"
    "v_accvgpr_read_b32 v244, a68\n"
    "v_mfma_f32_16x16x32_bf16 v[68:71], a[112:115], a[168:171], v[68:71]\n"
    "s_waitcnt lgkmcnt(2)\n"
    "buffer_store_dwordx4 v[252:255], v231, s[44:47], s17 offen offset:128 sc1\n"
    "v_mfma_f32_16x16x32_bf16 v[72:75], a[112:115], a[176:179], v[72:75]\n"
    "v_accvgpr_read_b32 v246, a69\n"
    "v_lshlrev_b32 v245, 16, v244\n"
    "v_mfma_f32_16x16x32_bf16 v[72:75], a[116:119], a[180:183], v[72:75]\n"
    "v_lshlrev_b32 v247, 16, v246\n"
    "v_and_b32 v244, s73, v244\n"
    "v_and_b32 v246, s73, v246\n"
    "v_mfma_f32_16x16x32_bf16 v[76:79], a[116:119], a[188:191], v[76:79]\n"
    "v_mul_f32 v245, v245, v132\n"
    "v_mul_f32 v247, v247, v196\n"
    "v_mfma_f32_16x16x32_bf16 v[76:79], a[112:115], a[184:187], v[76:79]\n"
    "v_mul_f32 v244, v244, v164\n"
    "v_mul_f32 v246, v246, v212\n"
    "v_mfma_f32_16x16x32_bf16 v[80:83], a[112:115], a[192:195], v[80:83]\n"
    "v_cvt_pk_bf16_f32 v236, v245, v244\n"
    "v_cvt_pk_bf16_f32 v237, v247, v246\n"
    "v_mfma_f32_16x16x32_bf16 v[80:83], a[116:119], a[196:199], v[80:83]\n"
    "v_accvgpr_read_b32 v244, a70\n"
    "v_accvgpr_read_b32 v246, a71\n"
    "v_lshlrev_b32 v245, 16, v244\n"
    "v_mfma_f32_16x16x32_bf16 v[84:87], a[116:119], a[204:207], v[84:87]\n"
    "v_lshlrev_b32 v247, 16, v246\n"
    "v_and_b32 v244, s73, v244\n"
    "v_mfma_f32_16x16x32_bf16 v[84:87], a[112:115], a[200:203], v[84:87]\n"
    "v_and_b32 v246, s73, v246\n"
    "v_mul_f32 v245, v245, v133\n"
    "v_mfma_f32_16x16x32_bf16 v[88:91], a[112:115], a[208:211], v[88:91]\n"
    "v_mul_f32 v247, v247, v197\n"
    "v_mul_f32 v244, v244, v165\n"
    "v_mfma_f32_16x16x32_bf16 v[88:91], a[116:119], a[212:215], v[88:91]\n"
    "ds_read_b128 v[240:243], v232 offset:2048\n"
    "v_mul_f32 v246, v246, v213\n"
    "v_cvt_pk_bf16_f32 v238, v245, v244\n"
    "v_mfma_f32_16x16x32_bf16 v[92:95], a[116:119], a[220:223], v[92:95]\n"
    "v_cvt_pk_bf16_f32 v239, v247, v246\n"
    "ds_write_b128 v228, v[236:239] offset:10240\n"
    "v_mfma_f32_16x16x32_bf16 v[92:95], a[112:115], a[216:219], v[92:95]\n"
    "buffer_load_dwordx4 a[112:115], v224, s[36:39], s60 offen offset:0 sc1\n"
    "s_waitcnt vmcnt(16)\n"
    "v_mfma_f32_16x16x32_bf16 a[224:227], a[120:123], a[160:163], a[224:227]\n"
    "ds_read_b128 a[160:163], v226 offset:49152\n"
    "buffer_load_dwordx4 a[116:119], v224, s[36:39], s60 offen offset:1024 sc1\n"
    "v_mfma_f32_16x16x32_bf16 a[224:227], a[124:127], a[164:167], a[224:227]\n"
    "ds_read_b128 a[164:167], v227 offset:49152\n"
    "s_waitcnt lgkmcnt(4)\n"
    "v_lshlrev_b32 v245, 16, v248\n"
    "v_lshlrev_b32 v247, 16, v249\n"
    "v_mfma_f32_16x16x32_bf16 a[228:231], a[124:127], a[172:175], a[228:231]\n"
    "ds_read_b128 a[172:175], v227 offset:51200\n"
    "v_and_b32 v244, s73, v248\n"
    "v_mfma_f32_16x16x32_bf16 a[228:231], a[120:123], a[168:171], a[228:231]\n"
    "ds_read_b128 a[168:171], v226 offset:51200\n"
    "v_and_b32 v246, s73, v249\n"
    "v_mfma_f32_16x16x32_bf16 a[232:235], a[120:123], a[176:179], a[232:235]\n"
    "ds_read_b128 a[176:179], v226 offset:53248\n"
    "v_mul_f32 v245, v245, v134\n"
    "v_mul_f32 v247, v247, v198\n"
    "v_mfma_f32_16x16x32_bf16 a[232:235], a[124:127], a[180:183], a[232:235]\n"
    "ds_read_b128 a[180:183], v227 offset:53248\n"
    "v_mul_f32 v244, v244, v166\n"
    "v_mfma_f32_16x16x32_bf16 a[236:239], a[124:127], a[188:191], a[236:239]\n"
    "ds_read_b128 a[188:191], v227 offset:55296\n"
    "v_mul_f32 v246, v246, v214\n"
    "v_mfma_f32_16x16x32_bf16 a[236:239], a[120:123], a[184:187], a[236:239]\n"
    "ds_read_b128 a[184:187], v226 offset:55296\n"
    "v_cvt_pk_bf16_f32 v236, v245, v244\n"
    "v_mfma_f32_16x16x32_bf16 v[96:99], a[120:123], a[192:195], v[96:99]\n"
    "ds_read_b128 a[192:195], v226 offset:57344\n"
    "v_cvt_pk_bf16_f32 v237, v247, v246\n"
    "v_lshlrev_b32 v245, 16, v250\n"
    "v_mfma_f32_16x16x32_bf16 v[96:99], a[124:127], a[196:199], v[96:99]\n"
    "ds_read_b128 a[196:199], v227 offset:57344\n"
    "v_lshlrev_b32 v247, 16, v251\n"
    "v_mfma_f32_16x16x32_bf16 v[100:103], a[124:127], a[204:207], v[100:103]\n"
    "ds_read_b128 a[204:207], v227 offset:59392\n"
    "v_and_b32 v244, s73, v250\n"
    "v_mfma_f32_16x16x32_bf16 v[100:103], a[120:123], a[200:203], v[100:103]\n"
    "ds_read_b128 a[200:203], v226 offset:59392\n"
    "v_and_b32 v246, s73, v251\n"
    "v_mfma_f32_16x16x32_bf16 v[104:107], a[120:123], a[208:211], v[104:107]\n"
    "ds_read_b128 a[208:211], v226 offset:61440\n"
    "v_mul_f32 v245, v245, v135\n"
    "v_mul_f32 v247, v247, v199\n"
    "v_mfma_f32_16x16x32_bf16 v[104:107], a[124:127], a[212:215], v[104:107]\n"
    "ds_read_b128 a[212:215], v227 offset:61440\n"
    "v_mul_f32 v244, v244, v167\n"
    "v_mfma_f32_16x16x32_bf16 v[108:111], a[124:127], a[220:223], v[108:111]\n"
    "ds_read_b128 a[220:223], v227 offset:63488\n"
    "v_mul_f32 v246, v246, v215\n"
    "v_mfma_f32_16x16x32_bf16 v[108:111], a[120:123], a[216:219], v[108:111]\n"
    "ds_read_b128 a[216:219], v226 offset:63488\n"
    "s_waitcnt vmcnt(15) lgkmcnt(8)\n"
    "v_mfma_f32_16x16x32_bf16 v[0:3], a[128:131], a[160:163], v[0:3]\n"
    "buffer_load_dwordx4 a[120:123], v224, s[36:39], s60 offen offset:2048 sc1\n"
    "v_cvt_pk_bf16_f32 v238, v245, v244\n"
    "v_mfma_f32_16x16x32_bf16 v[0:3], a[132:135], a[164:167], v[0:3]\n"
    "buffer_load_dwordx4 a[124:127], v224, s[36:39], s60 offen offset:3072 sc1\n"
    "v_mfma_f32_16x16x32_bf16 v[4:7], a[132:135], a[172:175], v[4:7]\n"
    "s_add_u32 s60, s60, 4352\n"
    "v_cvt_pk_bf16_f32 v239, v247, v246\n"
    "v_mfma_f32_16x16x32_bf16 v[4:7], a[128:131], a[168:171], v[4:7]\n"
    "buffer_store_dwordx4 v[240:243], v231, s[44:47], s18 offen offset:128 sc1\n"
    "v_mfma_f32_16x16x32_bf16 v[8:11], a[128:131], a[176:179], v[8:11]\n"
    "ds_read_b128 v[252:255], v232 offset:3072\n"
    "ds_write_b128 v229, v[236:239] offset:10240\n"
    "v_mfma_f32_16x16x32_bf16 v[8:11], a[132:135], a[180:183], v[8:11]\n"
    "ds_read_b128 v[248:251], v235 offset:6144\n"
    "v_accvgpr_read_b32 v244, a72\n"
    "v_mfma_f32_16x16x32_bf16 v[12:15], a[132:135], a[188:191], v[12:15]\n"
    "v_accvgpr_read_b32 v246, a73\n"
    "v_lshlrev_b32 v245, 16, v244\n"
    "v_mfma_f32_16x16x32_bf16 v[12:15], a[128:131], a[184:187], v[12:15]\n"
    "v_lshlrev_b32 v247, 16, v246\n"
    "v_and_b32 v244, s73, v244\n"
    "s_waitcnt lgkmcnt(3)\n"
    "v_mfma_f32_16x16x32_bf16 v[16:19], a[128:131], a[192:195], v[16:19]\n"
    "v_and_b32 v246, s73, v246\n"
    "v_mul_f32 v245, v245, v136\n"
    "v_mfma_f32_16x16x32_bf16 v[16:19], a[132:135], a[196:199], v[16:19]\n"
    "v_mul_f32 v247, v247, v200\n"
    "v_mul_f32 v244, v244, v168\n"
    "v_mfma_f32_16x16x32_bf16 v[20:23], a[132:135], a[204:207], v[20:23]\n"
    "v_mul_f32 v246, v246, v216\n"
    "v_cvt_pk_bf16_f32 v236, v245, v244\n"
    "v_mfma_f32_16x16x32_bf16 v[20:23], a[128:131], a[200:203], v[20:23]\n"
    "v_cvt_pk_bf16_f32 v237, v247, v246\n"
    "v_accvgpr_read_b32 v244, a74\n"
    "v_accvgpr_read_b32 v246, a75\n"
    "v_mfma_f32_16x16x32_bf16 v[24:27], a[128:131], a[208:211], v[24:27]\n"
    "v_lshlrev_b32 v245, 16, v244\n"
    "v_lshlrev_b32 v247, 16, v246\n"
    "v_mfma_f32_16x16x32_bf16 v[24:27], a[132:135], a[212:215], v[24:27]\n"
    "v_and_b32 v244, s73, v244\n"
    "v_and_b32 v246, s73, v246\n"
    "v_mfma_f32_16x16x32_bf16 v[28:31], a[132:135], a[220:223], v[28:31]\n"
    "s_waitcnt lgkmcnt(2)\n"
    "buffer_store_dwordx4 v[252:255], v231, s[44:47], s19 offen offset:128 sc1\n"
    "v_mfma_f32_16x16x32_bf16 v[28:31], a[128:131], a[216:219], v[28:31]\n"
    "v_mul_f32 v245, v245, v137\n"
    "v_mul_f32 v247, v247, v201\n"
    "s_waitcnt vmcnt(17)\n"
    "v_mfma_f32_16x16x32_bf16 v[32:35], a[136:139], a[160:163], v[32:35]\n"
    "buffer_load_dwordx4 a[128:131], v224, s[36:39], s60 offen offset:0 sc1\n"
    "v_mfma_f32_16x16x32_bf16 v[32:35], a[140:143], a[164:167], v[32:35]\n"
    "buffer_load_dwordx4 a[132:135], v224, s[36:39], s60 offen offset:1024 sc1\n"
    "v_mfma_f32_16x16x32_bf16 v[36:39], a[140:143], a[172:175], v[36:39]\n"
    "v_mul_f32 v244, v244, v169\n"
    "v_mul_f32 v246, v246, v217\n"
    "v_mfma_f32_16x16x32_bf16 v[36:39], a[136:139], a[168:171], v[36:39]\n"
    "v_cvt_pk_bf16_f32 v238, v245, v244\n"
    "v_cvt_pk_bf16_f32 v239, v247, v246\n"
    "ds_write_b128 v228, v[236:239] offset:12288\n"
    "v_mfma_f32_16x16x32_bf16 v[40:43], a[136:139], a[176:179], v[40:43]\n"
    "ds_read_b128 v[240:243], v232 offset:4096\n"
    "s_waitcnt lgkmcnt(2)\n"
    "v_lshlrev_b32 v245, 16, v248\n"
    "v_mfma_f32_16x16x32_bf16 v[40:43], a[140:143], a[180:183], v[40:43]\n"
    "v_lshlrev_b32 v247, 16, v249\n"
    "v_and_b32 v244, s73, v248\n"
    "v_mfma_f32_16x16x32_bf16 v[44:47], a[140:143], a[188:191], v[44:47]\n"
    "v_and_b32 v246, s73, v249\n"
    "v_mul_f32 v245, v245, v138\n"
    "v_mul_f32 v247, v247, v202\n"
    "v_mfma_f32_16x16x32_bf16 v[44:47], a[136:139], a[184:187], v[44:47]\n"
    "v_mul_f32 v244, v244, v170\n"
    "v_mul_f32 v246, v246, v218\n"
    "v_mfma_f32_16x16x32_bf16 v[48:51], a[136:139], a[192:195], v[48:51]\n"
    "v_cvt_pk_bf16_f32 v236, v245, v244\n"
    "v_cvt_pk_bf16_f32 v237, v247, v246\n"
    "v_mfma_f32_16x16x32_bf16 v[48:51], a[140:143], a[196:199], v[48:51]\n"
    "v_lshlrev_b32 v245, 16, v250\n"
    "v_lshlrev_b32 v247, 16, v251\n"
    "v_mfma_f32_16x16x32_bf16 v[52:55], a[140:143], a[204:207], v[52:55]\n"
    "v_and_b32 v244, s73, v250\n"
    "v_and_b32 v246, s73, v251\n"
    "v_mul_f32 v245, v245, v139\n"
    "v_mfma_f32_16x16x32_bf16 v[52:55], a[136:139], a[200:203], v[52:55]\n"
    "v_mul_f32 v247, v247, v203\n"
    "v_mul_f32 v244, v244, v171\n"
    "v_mfma_f32_16x16x32_bf16 v[56:59], a[136:139], a[208:211], v[56:59]\n"
    "v_mul_f32 v246, v246, v219\n"
    "v_cvt_pk_bf16_f32 v238, v245, v244\n"
    "v_mfma_f32_16x16x32_bf16 v[56:59], a[140:143], a[212:215], v[56:59]\n"
    "v_cvt_pk_bf16_f32 v239, v247, v246\n"
    "ds_write_b128 v229, v[236:239] offset:12288\n"
    "v_mfma_f32_16x16x32_bf16 v[60:63], a[140:143], a[220:223], v[60:63]\n"
    "s_waitcnt lgkmcnt(1)\n"
    "buffer_store_dwordx4 v[240:243], v231, s[44:47], s20 offen offset:128 sc1\n"
    "v_mfma_f32_16x16x32_bf16 v[60:63], a[136:139], a[216:219], v[60:63]\n"
    "ds_read_b128 v[248:251], v235 offset:7168\n"
    "s_waitcnt vmcnt(17)\n"
    "v_mfma_f32_16x16x32_bf16 v[64:67], a[144:147], a[160:163], v[64:67]\n"
    "buffer_load_dwordx4 a[136:139], v224, s[36:39], s60 offen offset:2048 sc1\n"
    "v_mfma_f32_16x16x32_bf16 v[64:67], a[148:151], a[164:167], v[64:67]\n"
    "buffer_load_dwordx4 a[140:143], v224, s[36:39], s60 offen offset:3072 sc1\n"
    "v_mfma_f32_16x16x32_bf16 v[68:71], a[148:151], a[172:175], v[68:71]\n"
    "s_add_u32 s60, s60, 4352\n"
    "v_accvgpr_read_b32 v244, a76\n"
    "v_accvgpr_read_b32 v246, a77\n"
    "v_mfma_f32_16x16x32_bf16 v[68:71], a[144:147], a[168:171], v[68:71]\n"
    "v_lshlrev_b32 v245, 16, v244\n"
    "v_lshlrev_b32 v247, 16, v246\n"
    "v_mfma_f32_16x16x32_bf16 v[72:75], a[144:147], a[176:179], v[72:75]\n"
    "v_and_b32 v244, s73, v244\n"
    "v_and_b32 v246, s73, v246\n"
    "v_mfma_f32_16x16x32_bf16 v[72:75], a[148:151], a[180:183], v[72:75]\n"
    "v_mul_f32 v245, v245, v140\n"
    "v_mul_f32 v247, v247, v204\n"
    "v_mul_f32 v244, v244, v172\n"
    "v_mfma_f32_16x16x32_bf16 v[76:79], a[148:151], a[188:191], v[76:79]\n"
    "v_mul_f32 v246, v246, v220\n"
    "v_cvt_pk_bf16_f32 v236, v245, v244\n"
    "v_mfma_f32_16x16x32_bf16 v[76:79], a[144:147], a[184:187], v[76:79]\n"
    "v_cvt_pk_bf16_f32 v237, v247, v246\n"
    "v_accvgpr_read_b32 v244, a78\n"
    "v_mfma_f32_16x16x32_bf16 v[80:83], a[144:147], a[192:195], v[80:83]\n"
    "v_accvgpr_read_b32 v246, a79\n"
    "v_lshlrev_b32 v245, 16, v244\n"
    "v_mfma_f32_16x16x32_bf16 v[80:83], a[148:151], a[196:199], v[80:83]\n"
    "v_lshlrev_b32 v247, 16, v246\n"
    "v_and_b32 v244, s73, v244\n"
    "v_and_b32 v246, s73, v246\n"
    "v_mfma_f32_16x16x32_bf16 v[84:87], a[148:151], a[204:207], v[84:87]\n"
    "v_mul_f32 v245, v245, v141\n"
    "v_mul_f32 v247, v247, v205\n"
    "v_mfma_f32_16x16x32_bf16 v[84:87], a[144:147], a[200:203], v[84:87]\n"
    "v_mul_f32 v244, v244, v173\n"
    "v_mul_f32 v246, v246, v221\n"
    "v_mfma_f32_16x16x32_bf16 v[88:91], a[144:147], a[208:211], v[88:91]\n"
    "v_cvt_pk_bf16_f32 v238, v245, v244\n"
    "v_cvt_pk_bf16_f32 v239, v247, v246\n"
    "v_mfma_f32_16x16x32_bf16 v[88:91], a[148:151], a[212:215], v[88:91]\n"
    "ds_write_b128 v228, v[236:239] offset:14336\n"
    "v_mfma_f32_16x16x32_bf16 v[92:95], a[148:151], a[220:223], v[92:95]\n"
    "s_waitcnt lgkmcnt(1)\n"
    "v_lshlrev_b32 v245, 16, v248\n"
    "v_lshlrev_b32 v247, 16, v249\n"
    "v_mfma_f32_16x16x32_bf16 v[92:95], a[144:147], a[216:219], v[92:95]\n"
    "buffer_load_dwordx4 a[144:147], v224, s[36:39], s60 offen offset:0 sc1\n"
    "s_waitcnt vmcnt(18)\n"
    "v_mfma_f32_16x16x32_bf16 a[224:227], a[152:155], a[160:163], a[224:227]\n"
    "ds_read_b128 a[160:163], v226 offset:0\n"
    "buffer_load_dwordx4 a[148:151], v224, s[36:39], s60 offen offset:1024 sc1\n"
    "v_mfma_f32_16x16x32_bf16 a[224:227], a[156:159], a[164:167], a[224:227]\n"
    "ds_read_b128 a[164:167], v227 offset:0\n"
    "v_and_b32 v244, s73, v248\n"
    "v_and_b32 v246, s73, v249\n"
    "v_mfma_f32_16x16x32_bf16 a[228:231], a[156:159], a[172:175], a[228:231]\n"
    "ds_read_b128 a[172:175], v227 offset:2048\n"
    "v_mul_f32 v245, v245, v142\n"
    "v_mfma_f32_16x16x32_bf16 a[228:231], a[152:155], a[168:171], a[228:231]\n"
    "ds_read_b128 a[168:171], v226 offset:2048\n"
    "v_mul_f32 v247, v247, v206\n"
    "v_mfma_f32_16x16x32_bf16 a[232:235], a[152:155], a[176:179], a[232:235]\n"
    "ds_read_b128 a[176:179], v226 offset:4096\n"
    "v_mul_f32 v244, v244, v174\n"
    "v_mul_f32 v246, v246, v222\n"
    "v_mfma_f32_16x16x32_bf16 a[232:235], a[156:159], a[180:183], a[232:235]\n"
    "ds_read_b128 a[180:183], v227 offset:4096\n"
    "v_cvt_pk_bf16_f32 v236, v245, v244\n"
    "v_mfma_f32_16x16x32_bf16 a[236:239], a[156:159], a[188:191], a[236:239]\n"
    "ds_read_b128 a[188:191], v227 offset:6144\n"
    "v_cvt_pk_bf16_f32 v237, v247, v246\n"
    "v_mfma_f32_16x16x32_bf16 a[236:239], a[152:155], a[184:187], a[236:239]\n"
    "ds_read_b128 a[184:187], v226 offset:6144\n"
    "v_lshlrev_b32 v245, 16, v250\n"
    "v_mfma_f32_16x16x32_bf16 v[96:99], a[152:155], a[192:195], v[96:99]\n"
    "ds_read_b128 a[192:195], v226 offset:8192\n"
    "v_lshlrev_b32 v247, 16, v251\n"
    "v_and_b32 v244, s73, v250\n"
    "v_mfma_f32_16x16x32_bf16 v[96:99], a[156:159], a[196:199], v[96:99]\n"
    "ds_read_b128 a[196:199], v227 offset:8192\n"
    "v_and_b32 v246, s73, v251\n"
    "v_mfma_f32_16x16x32_bf16 v[100:103], a[156:159], a[204:207], v[100:103]\n"
    "ds_read_b128 a[204:207], v227 offset:10240\n"
    "v_mul_f32 v245, v245, v143\n"
    "v_mfma_f32_16x16x32_bf16 v[100:103], a[152:155], a[200:203], v[100:103]\n"
    "ds_read_b128 a[200:203], v226 offset:10240\n"
    "v_mul_f32 v247, v247, v207\n"
    "v_mfma_f32_16x16x32_bf16 v[104:107], a[152:155], a[208:211], v[104:107]\n"
    "ds_read_b128 a[208:211], v226 offset:12288\n"
    "v_mul_f32 v244, v244, v175\n"
    "v_mul_f32 v246, v246, v223\n"
    "v_mfma_f32_16x16x32_bf16 v[104:107], a[156:159], a[212:215], v[104:107]\n"
    "ds_read_b128 a[212:215], v227 offset:12288\n"
    "v_cvt_pk_bf16_f32 v238, v245, v244\n"
    "v_mfma_f32_16x16x32_bf16 v[108:111], a[156:159], a[220:223], v[108:111]\n"
    "ds_read_b128 a[220:223], v227 offset:14336\n"
    "v_cvt_pk_bf16_f32 v239, v247, v246\n"
    "v_mfma_f32_16x16x32_bf16 v[108:111], a[152:155], a[216:219], v[108:111]\n"
    "ds_read_b128 a[216:219], v226 offset:14336\n"
    "ds_write_b128 v229, v[236:239] offset:14336\n"
    "s_waitcnt vmcnt(16) lgkmcnt(9)\n"
    "v_mfma_f32_16x16x32_bf16 v[112:115], a[96:99], a[160:163], v[112:115]\n"
    "buffer_load_dwordx4 a[152:155], v224, s[36:39], s60 offen offset:2048 sc1\n"
    "v_mfma_f32_16x16x32_bf16 v[112:115], a[100:103], a[164:167], v[112:115]\n"
    "buffer_load_dwordx4 a[156:159], v224, s[36:39], s60 offen offset:3072 sc1\n"
    "v_mfma_f32_16x16x32_bf16 v[116:119], a[100:103], a[172:175], v[116:119]\n"
    "s_add_u32 s60, s60, 4352\n"
    "v_lshrrev_b32 v228, 3, v224\n"
    "v_mfma_f32_16x16x32_bf16 v[116:119], a[96:99], a[168:171], v[116:119]\n"
    "ds_read_b128 v[236:239], v232 offset:5120\n"
    "v_mfma_f32_16x16x32_bf16 v[120:123], a[96:99], a[176:179], v[120:123]\n"
    "v_and_b32 v228, 0x60, v228\n"
    "v_mfma_f32_16x16x32_bf16 v[120:123], a[100:103], a[180:183], v[120:123]\n"
    "v_mfma_f32_16x16x32_bf16 v[124:127], a[100:103], a[188:191], v[124:127]\n"
    "buffer_load_dwordx4 v[252:255], v228, s[56:59], s74 offen offset:0\n"
    "v_mfma_f32_16x16x32_bf16 v[124:127], a[96:99], a[184:187], v[124:127]\n"
    "s_waitcnt lgkmcnt(2)\n"
    "v_mfma_f32_16x16x32_bf16 v[128:131], a[96:99], a[192:195], v[128:131]\n"
    "ds_read_b128 v[240:243], v232 offset:6144\n"
    "v_mfma_f32_16x16x32_bf16 v[128:131], a[100:103], a[196:199], v[128:131]\n"
    "v_mfma_f32_16x16x32_bf16 v[132:135], a[100:103], a[204:207], v[132:135]\n"
    "v_mfma_f32_16x16x32_bf16 v[132:135], a[96:99], a[200:203], v[132:135]\n"
    "s_mov_b32 s40, s44\n"
    "s_mov_b32 s41, s45\n"
    "v_mfma_f32_16x16x32_bf16 v[136:139], a[96:99], a[208:211], v[136:139]\n"
    "v_mfma_f32_16x16x32_bf16 v[136:139], a[100:103], a[212:215], v[136:139]\n"
    "s_waitcnt lgkmcnt(1)\n"
    "buffer_store_dwordx4 v[236:239], v231, s[44:47], s21 offen offset:128 sc1\n"
    "v_mfma_f32_16x16x32_bf16 v[140:143], a[100:103], a[220:223], v[140:143]\n"
    "v_mfma_f32_16x16x32_bf16 v[140:143], a[96:99], a[216:219], v[140:143]\n"
    "s_waitcnt vmcnt(18)\n"
    "v_mfma_f32_16x16x32_bf16 v[144:147], a[104:107], a[160:163], v[144:147]\n"
    "buffer_load_dwordx4 a[96:99], v224, s[36:39], s60 offen offset:0 sc1\n"
    "v_mfma_f32_16x16x32_bf16 v[144:147], a[108:111], a[164:167], v[144:147]\n"
    "buffer_load_dwordx4 a[100:103], v224, s[36:39], s60 offen offset:1024 sc1\n"
    "v_mfma_f32_16x16x32_bf16 v[148:151], a[108:111], a[172:175], v[148:151]\n"
    "s_waitcnt lgkmcnt(0)\n"
    "buffer_store_dwordx4 v[240:243], v231, s[44:47], s22 offen offset:128 sc1\n"
    "v_mfma_f32_16x16x32_bf16 v[148:151], a[104:107], a[168:171], v[148:151]\n"
    "v_mfma_f32_16x16x32_bf16 v[152:155], a[104:107], a[176:179], v[152:155]\n"
    "s_mov_b32 s64, s84\n"
    "s_mov_b32 s62, 4\n"
    "v_mfma_f32_16x16x32_bf16 v[152:155], a[108:111], a[180:183], v[152:155]\n"
    "v_mfma_f32_16x16x32_bf16 v[156:159], a[108:111], a[188:191], v[156:159]\n"
    "v_mfma_f32_16x16x32_bf16 v[156:159], a[104:107], a[184:187], v[156:159]\n"
    "v_mfma_f32_16x16x32_bf16 v[160:163], a[104:107], a[192:195], v[160:163]\n"
    "v_mfma_f32_16x16x32_bf16 v[160:163], a[108:111], a[196:199], v[160:163]\n"
    "s_mov_b32 s63, 65536\n"
    "s_mov_b32 s67, 0\n"
    "v_mfma_f32_16x16x32_bf16 v[164:167], a[108:111], a[204:207], v[164:167]\n"
    "v_mfma_f32_16x16x32_bf16 v[164:167], a[104:107], a[200:203], v[164:167]\n"
    "v_mfma_f32_16x16x32_bf16 v[168:171], a[104:107], a[208:211], v[168:171]\n"
    "v_mfma_f32_16x16x32_bf16 v[168:171], a[108:111], a[212:215], v[168:171]\n"
    "s_mov_b32 s78, s80\n"
    "s_mov_b32 s79, 1\n"
    "v_mfma_f32_16x16x32_bf16 v[172:175], a[108:111], a[220:223], v[172:175]\n"
    "ds_read_b128 v[236:239], v232 offset:7168\n"
    "v_mfma_f32_16x16x32_bf16 v[172:175], a[104:107], a[216:219], v[172:175]\n"
    "s_waitcnt vmcnt(18)\n"
    "v_mfma_f32_16x16x32_bf16 v[176:179], a[112:115], a[160:163], v[176:179]\n"
    "buffer_load_dwordx4 a[104:107], v224, s[36:39], s60 offen offset:2048 sc1\n"
    "v_mfma_f32_16x16x32_bf16 v[176:179], a[116:119], a[164:167], v[176:179]\n"
    "buffer_load_dwordx4 a[108:111], v224, s[36:39], s60 offen offset:3072 sc1\n"
    "v_mfma_f32_16x16x32_bf16 v[180:183], a[116:119], a[172:175], v[180:183]\n"
    "s_add_u32 s60, s60, 4352\n"
    "s_mov_b32 s16, 30\n"
    "v_mfma_f32_16x16x32_bf16 v[180:183], a[112:115], a[168:171], v[180:183]\n"
    "v_mfma_f32_16x16x32_bf16 v[184:187], a[112:115], a[176:179], v[184:187]\n"
    "v_mfma_f32_16x16x32_bf16 v[184:187], a[116:119], a[180:183], v[184:187]\n"
    "v_mfma_f32_16x16x32_bf16 v[188:191], a[116:119], a[188:191], v[188:191]\n"
    "s_add_u32 s80, s80, 1\n"
    "v_mfma_f32_16x16x32_bf16 v[188:191], a[112:115], a[184:187], v[188:191]\n"
    "v_mfma_f32_16x16x32_bf16 v[192:195], a[112:115], a[192:195], v[192:195]\n"
    "s_waitcnt lgkmcnt(0)\n"
    "buffer_store_dwordx4 v[236:239], v231, s[44:47], s23 offen offset:128 sc1\n"
    "v_mfma_f32_16x16x32_bf16 v[192:195], a[116:119], a[196:199], v[192:195]\n"
    "v_mfma_f32_16x16x32_bf16 v[196:199], a[116:119], a[204:207], v[196:199]\n"
    "v_mfma_f32_16x16x32_bf16 v[196:199], a[112:115], a[200:203], v[196:199]\n"
    "v_mfma_f32_16x16x32_bf16 v[200:203], a[112:115], a[208:211], v[200:203]\n"
    "v_mfma_f32_16x16x32_bf16 v[200:203], a[116:119], a[212:215], v[200:203]\n"
    "v_mfma_f32_16x16x32_bf16 v[204:207], a[116:119], a[220:223], v[204:207]\n"
    "v_mfma_f32_16x16x32_bf16 v[204:207], a[112:115], a[216:219], v[204:207]\n"
    "buffer_load_dwordx4 a[112:115], v224, s[36:39], s60 offen offset:0 sc1\n"
    "s_waitcnt vmcnt(20)\n"
    "v_mfma_f32_16x16x32_bf16 a[240:243], a[120:123], a[160:163], a[240:243]\n"
    "ds_read_b128 a[160:163], v226 offset:16384\n"
    "buffer_load_dwordx4 a[116:119], v224, s[36:39], s60 offen offset:1024 sc1\n"
    "v_mfma_f32_16x16x32_bf16 a[240:243], a[124:127], a[164:167], a[240:243]\n"
    "ds_read_b128 a[164:167], v227 offset:16384\n"
    "v_mfma_f32_16x16x32_bf16 a[244:247], a[124:127], a[172:175], a[244:247]\n"
    "ds_read_b128 a[172:175], v227 offset:18432\n"
    "v_mfma_f32_16x16x32_bf16 a[244:247], a[120:123], a[168:171], a[244:247]\n"
    "ds_read_b128 a[168:171], v226 offset:18432\n"
    "v_mfma_f32_16x16x32_bf16 a[248:251], a[120:123], a[176:179], a[248:251]\n"
    "ds_read_b128 a[176:179], v226 offset:20480\n"
    "v_mfma_f32_16x16x32_bf16 a[248:251], a[124:127], a[180:183], a[248:251]\n"
    "ds_read_b128 a[180:183], v227 offset:20480\n"
    "v_mfma_f32_16x16x32_bf16 a[252:255], a[124:127], a[188:191], a[252:255]\n"
    "ds_read_b128 a[188:191], v227 offset:22528\n"
    "v_mfma_f32_16x16x32_bf16 a[252:255], a[120:123], a[184:187], a[252:255]\n"
    "ds_read_b128 a[184:187], v226 offset:22528\n"
    "v_mfma_f32_16x16x32_bf16 v[208:211], a[120:123], a[192:195], v[208:211]\n"
    "ds_read_b128 a[192:195], v226 offset:24576\n"
    "v_mfma_f32_16x16x32_bf16 v[208:211], a[124:127], a[196:199], v[208:211]\n"
    "ds_read_b128 a[196:199], v227 offset:24576\n"
    "v_mfma_f32_16x16x32_bf16 v[212:215], a[124:127], a[204:207], v[212:215]\n"
    "ds_read_b128 a[204:207], v227 offset:26624\n"
    "v_mfma_f32_16x16x32_bf16 v[212:215], a[120:123], a[200:203], v[212:215]\n"
    "ds_read_b128 a[200:203], v226 offset:26624\n"
    "v_mfma_f32_16x16x32_bf16 v[216:219], a[120:123], a[208:211], v[216:219]\n"
    "ds_read_b128 a[208:211], v226 offset:28672\n"
    "v_mfma_f32_16x16x32_bf16 v[216:219], a[124:127], a[212:215], v[216:219]\n"
    "ds_read_b128 a[212:215], v227 offset:28672\n"
    "v_mfma_f32_16x16x32_bf16 v[220:223], a[124:127], a[220:223], v[220:223]\n"
    "ds_read_b128 a[220:223], v227 offset:30720\n"
    "v_mfma_f32_16x16x32_bf16 v[220:223], a[120:123], a[216:219], v[220:223]\n"
    "ds_read_b128 a[216:219], v226 offset:30720\n"
    "s_waitcnt vmcnt(17) lgkmcnt(8)\n"
    "v_mfma_f32_16x16x32_bf16 v[112:115], a[128:131], a[160:163], v[112:115]\n"
    "buffer_load_dwordx4 a[120:123], v224, s[36:39], s60 offen offset:2048 sc1\n"
    "v_mfma_f32_16x16x32_bf16 v[112:115], a[132:135], a[164:167], v[112:115]\n"
    "buffer_load_dwordx4 a[124:127], v224, s[36:39], s60 offen offset:3072 sc1\n"
    "v_mfma_f32_16x16x32_bf16 v[116:119], a[132:135], a[172:175], v[116:119]\n"
    "s_add_u32 s60, s60, 4352\n"
    "s_waitcnt vmcnt(11)\n"
    "v_lshlrev_b32 v244, 16, v252\n"
    "v_mfma_f32_16x16x32_bf16 v[116:119], a[128:131], a[168:171], v[116:119]\n"
    "ds_read_b128 v[240:243], v232 offset:8192\n"
    "v_mfma_f32_16x16x32_bf16 v[120:123], a[128:131], a[176:179], v[120:123]\n"
    "v_and_b32 v245, s73, v252\n"
    "v_lshlrev_b32 v246, 16, v253\n"
    "v_mfma_f32_16x16x32_bf16 v[120:123], a[132:135], a[180:183], v[120:123]\n"
    "v_and_b32 v247, s73, v253\n"
    "v_lshlrev_b32 v248, 16, v254\n"
    "v_mfma_f32_16x16x32_bf16 v[124:127], a[132:135], a[188:191], v[124:127]\n"
    "v_and_b32 v249, s73, v254\n"
    "v_lshlrev_b32 v250, 16, v255\n"
    "v_mfma_f32_16x16x32_bf16 v[124:127], a[128:131], a[184:187], v[124:127]\n"
    "s_waitcnt lgkmcnt(1)\n"
    "v_mfma_f32_16x16x32_bf16 v[128:131], a[128:131], a[192:195], v[128:131]\n"
    "v_and_b32 v251, s73, v255\n"
    "v_mfma_f32_16x16x32_bf16 v[128:131], a[132:135], a[196:199], v[128:131]\n"
    "buffer_load_dwordx4 v[252:255], v228, s[56:59], s74 offen offset:16\n"
    "v_mfma_f32_16x16x32_bf16 v[132:135], a[132:135], a[204:207], v[132:135]\n"
    "v_add_f32 v0, v244, v0\n"
    "v_add_f32 v32, v245, v32\n"
    "v_mfma_f32_16x16x32_bf16 v[132:135], a[128:131], a[200:203], v[132:135]\n"
    "v_mfma_f32_16x16x32_bf16 v[136:139], a[128:131], a[208:211], v[136:139]\n"
    "ds_read_b128 v[236:239], v232 offset:9216\n"
    "v_add_f32 v64, v246, v64\n"
    "v_mfma_f32_16x16x32_bf16 v[136:139], a[132:135], a[212:215], v[136:139]\n"
    "s_waitcnt lgkmcnt(1)\n"
    "buffer_store_dwordx4 v[240:243], v231, s[44:47], s24 offen offset:128 sc1\n"
    "v_mfma_f32_16x16x32_bf16 v[140:143], a[132:135], a[220:223], v[140:143]\n"
    "v_accvgpr_read_b32 v229, a224\n"
    "v_add_f32 v229, v247, v229\n"
    "v_mfma_f32_16x16x32_bf16 v[140:143], a[128:131], a[216:219], v[140:143]\n"
    "v_mfma_f32_16x16x32_bf16 v[144:147], a[136:139], a[160:163], v[144:147]\n"
    "buffer_load_dwordx4 a[128:131], v224, s[36:39], s60 offen offset:0 sc1\n"
    "v_mfma_f32_16x16x32_bf16 v[144:147], a[140:143], a[164:167], v[144:147]\n"
    "buffer_load_dwordx4 a[132:135], v224, s[36:39], s60 offen offset:1024 sc1\n"
    "v_mfma_f32_16x16x32_bf16 v[148:151], a[140:143], a[172:175], v[148:151]\n"
    "v_mfma_f32_16x16x32_bf16 v[148:151], a[136:139], a[168:171], v[148:151]\n"
    "v_accvgpr_write_b32 a224, v229\n"
    "v_add_f32 v1, v248, v1\n"
    "v_mfma_f32_16x16x32_bf16 v[152:155], a[136:139], a[176:179], v[152:155]\n"
    "v_add_f32 v33, v249, v33\n"
    "v_add_f32 v65, v250, v65\n"
    "v_mfma_f32_16x16x32_bf16 v[152:155], a[140:143], a[180:183], v[152:155]\n"
    "v_accvgpr_read_b32 v229, a225\n"
    "v_add_f32 v229, v251, v229\n"
    "v_mfma_f32_16x16x32_bf16 v[156:159], a[140:143], a[188:191], v[156:159]\n"
    "s_waitcnt lgkmcnt(0)\n"
    "buffer_store_dwordx4 v[236:239], v231, s[44:47], s25 offen offset:128 sc1\n"
    "v_mfma_f32_16x16x32_bf16 v[156:159], a[136:139], a[184:187], v[156:159]\n"
    "v_accvgpr_write_b32 a225, v229\n"
    "v_add_f32 v4, v244, v4\n"
    "v_mfma_f32_16x16x32_bf16 v[160:163], a[136:139], a[192:195], v[160:163]\n"
    "v_add_f32 v36, v245, v36\n"
    "v_add_f32 v68, v246, v68\n"
    "v_mfma_f32_16x16x32_bf16 v[160:163], a[140:143], a[196:199], v[160:163]\n"
    "v_accvgpr_read_b32 v229, a228\n"
    "v_add_f32 v229, v247, v229\n"
    "v_mfma_f32_16x16x32_bf16 v[164:167], a[140:143], a[204:207], v[164:167]\n"
    "v_mfma_f32_16x16x32_bf16 v[164:167], a[136:139], a[200:203], v[164:167]\n"
    "v_accvgpr_write_b32 a228, v229\n"
    "v_add_f32 v5, v248, v5\n"
    "v_mfma_f32_16x16x32_bf16 v[168:171], a[136:139], a[208:211], v[168:171]\n"
    "v_add_f32 v37, v249, v37\n"
    "v_add_f32 v69, v250, v69\n"
    "v_mfma_f32_16x16x32_bf16 v[168:171], a[140:143], a[212:215], v[168:171]\n"
    "v_accvgpr_read_b32 v229, a229\n"
    "v_add_f32 v229, v251, v229\n"
    "v_mfma_f32_16x16x32_bf16 v[172:175], a[140:143], a[220:223], v[172:175]\n"
    "v_mfma_f32_16x16x32_bf16 v[172:175], a[136:139], a[216:219], v[172:175]\n"
    "v_accvgpr_write_b32 a229, v229\n"
    "v_mfma_f32_16x16x32_bf16 v[176:179], a[144:147], a[160:163], v[176:179]\n"
    "buffer_load_dwordx4 a[136:139], v224, s[36:39], s60 offen offset:2048 sc1\n"
    "v_mfma_f32_16x16x32_bf16 v[176:179], a[148:151], a[164:167], v[176:179]\n"
    "buffer_load_dwordx4 a[140:143], v224, s[36:39], s60 offen offset:3072 sc1\n"
    "v_mfma_f32_16x16x32_bf16 v[180:183], a[148:151], a[172:175], v[180:183]\n"
    "s_add_u32 s60, s60, 4352\n"
    "v_add_f32 v8, v244, v8\n"
    "v_mfma_f32_16x16x32_bf16 v[180:183], a[144:147], a[168:171], v[180:183]\n"
    "ds_read_b128 v[240:243], v232 offset:10240\n"
    "v_add_f32 v40, v245, v40\n"
    "v_mfma_f32_16x16x32_bf16 v[184:187], a[144:147], a[176:179], v[184:187]\n"
    "v_add_f32 v72, v246, v72\n"
    "v_accvgpr_read_b32 v229, a232\n"
    "v_mfma_f32_16x16x32_bf16 v[184:187], a[148:151], a[180:183], v[184:187]\n"
    "v_mfma_f32_16x16x32_bf16 v[188:191], a[148:151], a[188:191], v[188:191]\n"
    "v_add_f32 v229, v247, v229\n"
    "v_accvgpr_write_b32 a232, v229\n"
    "v_mfma_f32_16x16x32_bf16 v[188:191], a[144:147], a[184:187], v[188:191]\n"
    "v_add_f32 v9, v248, v9\n"
    "v_add_f32 v41, v249, v41\n"
    "v_mfma_f32_16x16x32_bf16 v[192:195], a[144:147], a[192:195], v[192:195]\n"
    "v_add_f32 v73, v250, v73\n"
    "v_accvgpr_read_b32 v229, a233\n"
    "v_mfma_f32_16x16x32_bf16 v[192:195], a[148:151], a[196:199], v[192:195]\n"
    "v_mfma_f32_16x16x32_bf16 v[196:199], a[148:151], a[204:207], v[196:199]\n"
    "v_add_f32 v229, v251, v229\n"
    "v_accvgpr_write_b32 a233, v229\n"
    "v_mfma_f32_16x16x32_bf16 v[196:199], a[144:147], a[200:203], v[196:199]\n"
    "v_add_f32 v12, v244, v12\n"
    "v_add_f32 v44, v245, v44\n"
    "v_mfma_f32_16x16x32_bf16 v[200:203], a[144:147], a[208:211], v[200:203]\n"
    "v_add_f32 v76, v246, v76\n"
    "v_accvgpr_read_b32 v229, a236\n"
    "v_mfma_f32_16x16x32_bf16 v[200:203], a[148:151], a[212:215], v[200:203]\n"
    "s_waitcnt lgkmcnt(0)\n"
    "buffer_store_dwordx4 v[240:243], v231, s[44:47], s26 offen offset:128 sc1\n"
    "v_mfma_f32_16x16x32_bf16 v[204:207], a[148:151], a[220:223], v[204:207]\n"
    "v_add_f32 v229, v247, v229\n"
    "v_accvgpr_write_b32 a236, v229\n"
    "v_mfma_f32_16x16x32_bf16 v[204:207], a[144:147], a[216:219], v[204:207]\n"
    "buffer_load_dwordx4 a[144:147], v224, s[36:39], s60 offen offset:0 sc1\n"
    "v_mfma_f32_16x16x32_bf16 a[240:243], a[152:155], a[160:163], a[240:243]\n"
    "ds_read_b128 a[160:163], v226 offset:32768\n"
    "buffer_load_dwordx4 a[148:151], v224, s[36:39], s60 offen offset:1024 sc1\n"
    "v_mfma_f32_16x16x32_bf16 a[240:243], a[156:159], a[164:167], a[240:243]\n"
    "ds_read_b128 a[164:167], v227 offset:32768\n"
    "v_add_f32 v13, v248, v13\n"
    "v_mfma_f32_16x16x32_bf16 a[244:247], a[156:159], a[172:175], a[244:247]\n"
    "ds_read_b128 a[172:175], v227 offset:34816\n"
    "v_add_f32 v45, v249, v45\n"
    "v_mfma_f32_16x16x32_bf16 a[244:247], a[152:155], a[168:171], a[244:247]\n"
    "ds_read_b128 a[168:171], v226 offset:34816\n"
    "v_add_f32 v77, v250, v77\n"
    "v_mfma_f32_16x16x32_bf16 a[248:251], a[152:155], a[176:179], a[248:251]\n"
    "ds_read_b128 a[176:179], v226 offset:36864\n"
    "v_mfma_f32_16x16x32_bf16 a[248:251], a[156:159], a[180:183], a[248:251]\n"
    "ds_read_b128 a[180:183], v227 offset:36864\n"
    "v_accvgpr_read_b32 v229, a237\n"
    "v_mfma_f32_16x16x32_bf16 a[252:255], a[156:159], a[188:191], a[252:255]\n"
    "ds_read_b128 a[188:191], v227 offset:38912\n"
    "v_add_f32 v229, v251, v229\n"
    "v_mfma_f32_16x16x32_bf16 a[252:255], a[152:155], a[184:187], a[252:255]\n"
    "ds_read_b128 a[184:187], v226 offset:38912\n"
    "v_accvgpr_write_b32 a237, v229\n"
    "v_mfma_f32_16x16x32_bf16 v[208:211], a[152:155], a[192:195], v[208:211]\n"
    "ds_read_b128 a[192:195], v226 offset:40960\n"
    "v_mfma_f32_16x16x32_bf16 v[208:211], a[156:159], a[196:199], v[208:211]\n"
    "ds_read_b128 a[196:199], v227 offset:40960\n"
    "v_add_f32 v16, v244, v16\n"
    "v_mfma_f32_16x16x32_bf16 v[212:215], a[156:159], a[204:207], v[212:215]\n"
    "ds_read_b128 a[204:207], v227 offset:43008\n"
    "v_add_f32 v48, v245, v48\n"
    "v_mfma_f32_16x16x32_bf16 v[212:215], a[152:155], a[200:203], v[212:215]\n"
    "ds_read_b128 a[200:203], v226 offset:43008\n"
    "v_add_f32 v80, v246, v80\n"
    "v_mfma_f32_16x16x32_bf16 v[216:219], a[152:155], a[208:211], v[216:219]\n"
    "ds_read_b128 a[208:211], v226 offset:45056\n"
    "v_mfma_f32_16x16x32_bf16 v[216:219], a[156:159], a[212:215], v[216:219]\n"
    "ds_read_b128 a[212:215], v227 offset:45056\n"
    "v_add_f32 v96, v247, v96\n"
    "v_mfma_f32_16x16x32_bf16 v[220:223], a[156:159], a[220:223], v[220:223]\n"
    "ds_read_b128 a[220:223], v227 offset:47104\n"
    "v_add_f32 v17, v248, v17\n"
    "v_mfma_f32_16x16x32_bf16 v[220:223], a[152:155], a[216:219], v[220:223]\n"
    "ds_read_b128 a[216:219], v226 offset:47104\n"
    "s_waitcnt vmcnt(18) lgkmcnt(8)\n"
    "v_mfma_f32_16x16x32_bf16 v[112:115], a[96:99], a[160:163], v[112:115]\n"
    "buffer_load_dwordx4 a[152:155], v224, s[36:39], s60 offen offset:2048 sc1\n"
    "v_mfma_f32_16x16x32_bf16 v[112:115], a[100:103], a[164:167], v[112:115]\n"
    "buffer_load_dwordx4 a[156:159], v224, s[36:39], s60 offen offset:3072 sc1\n"
    "v_mfma_f32_16x16x32_bf16 v[116:119], a[100:103], a[172:175], v[116:119]\n"
    "s_add_u32 s60, s60, 4352\n"
    "v_add_f32 v49, v249, v49\n"
    "v_mfma_f32_16x16x32_bf16 v[116:119], a[96:99], a[168:171], v[116:119]\n"
    "ds_read_b128 v[236:239], v232 offset:11264\n"
    "v_mfma_f32_16x16x32_bf16 v[120:123], a[96:99], a[176:179], v[120:123]\n"
    "v_add_f32 v81, v250, v81\n"
    "v_add_f32 v97, v251, v97\n"
    "v_mfma_f32_16x16x32_bf16 v[120:123], a[100:103], a[180:183], v[120:123]\n"
    "v_add_f32 v20, v244, v20\n"
    "v_add_f32 v52, v245, v52\n"
    "v_mfma_f32_16x16x32_bf16 v[124:127], a[100:103], a[188:191], v[124:127]\n"
    "v_add_f32 v84, v246, v84\n"
    "v_add_f32 v100, v247, v100\n"
    "v_mfma_f32_16x16x32_bf16 v[124:127], a[96:99], a[184:187], v[124:127]\n"
    "s_waitcnt lgkmcnt(1)\n"
    "v_mfma_f32_16x16x32_bf16 v[128:131], a[96:99], a[192:195], v[128:131]\n"
    "v_add_f32 v21, v248, v21\n"
    "v_add_f32 v53, v249, v53\n"
    "v_mfma_f32_16x16x32_bf16 v[128:131], a[100:103], a[196:199], v[128:131]\n"
    "v_add_f32 v85, v250, v85\n"
    "v_add_f32 v101, v251, v101\n"
    "v_mfma_f32_16x16x32_bf16 v[132:135], a[100:103], a[204:207], v[132:135]\n"
    "v_add_f32 v24, v244, v24\n"
    "v_add_f32 v56, v245, v56\n"
    "v_mfma_f32_16x16x32_bf16 v[132:135], a[96:99], a[200:203], v[132:135]\n"
    "v_mfma_f32_16x16x32_bf16 v[136:139], a[96:99], a[208:211], v[136:139]\n"
    "buffer_load_dword v233, v224, s[48:51], 0 offen offset:64\n"
    "v_mfma_f32_16x16x32_bf16 v[136:139], a[100:103], a[212:215], v[136:139]\n"
    "s_waitcnt lgkmcnt(0)\n"
    "buffer_store_dwordx4 v[236:239], v231, s[44:47], s27 offen offset:128 sc1\n"
    "v_mfma_f32_16x16x32_bf16 v[140:143], a[100:103], a[220:223], v[140:143]\n"
    "v_add_f32 v88, v246, v88\n"
    "v_add_f32 v104, v247, v104\n"
    "v_mfma_f32_16x16x32_bf16 v[140:143], a[96:99], a[216:219], v[140:143]\n"
    "s_waitcnt vmcnt(19)\n"
    "v_mfma_f32_16x16x32_bf16 v[144:147], a[104:107], a[160:163], v[144:147]\n"
    "buffer_load_dwordx4 a[96:99], v224, s[36:39], s60 offen offset:0 sc1\n"
    "v_mfma_f32_16x16x32_bf16 v[144:147], a[108:111], a[164:167], v[144:147]\n"
    "buffer_load_dwordx4 a[100:103], v224, s[36:39], s60 offen offset:1024 sc1\n"
    "v_mfma_f32_16x16x32_bf16 v[148:151], a[108:111], a[172:175], v[148:151]\n"
    "ds_read_b128 v[240:243], v232 offset:12288\n"
    "v_mfma_f32_16x16x32_bf16 v[148:151], a[104:107], a[168:171], v[148:151]\n"
    "v_add_f32 v25, v248, v25\n"
    "v_add_f32 v57, v249, v57\n"
    "v_mfma_f32_16x16x32_bf16 v[152:155], a[104:107], a[176:179], v[152:155]\n"
    "v_add_f32 v89, v250, v89\n"
    "v_add_f32 v105, v251, v105\n"
    "v_mfma_f32_16x16x32_bf16 v[152:155], a[108:111], a[180:183], v[152:155]\n"
    "v_add_f32 v28, v244, v28\n"
    "v_add_f32 v60, v245, v60\n"
    "v_mfma_f32_16x16x32_bf16 v[156:159], a[108:111], a[188:191], v[156:159]\n"
    "v_mfma_f32_16x16x32_bf16 v[156:159], a[104:107], a[184:187], v[156:159]\n"
    "v_add_f32 v92, v246, v92\n"
    "v_add_f32 v108, v247, v108\n"
    "v_mfma_f32_16x16x32_bf16 v[160:163], a[104:107], a[192:195], v[160:163]\n"
    "v_add_f32 v29, v248, v29\n"
    "v_add_f32 v61, v249, v61\n"
    "v_mfma_f32_16x16x32_bf16 v[160:163], a[108:111], a[196:199], v[160:163]\n"
    "v_add_f32 v93, v250, v93\n"
    "v_add_f32 v109, v251, v109\n"
    "v_mfma_f32_16x16x32_bf16 v[164:167], a[108:111], a[204:207], v[164:167]\n"
    "v_mfma_f32_16x16x32_bf16 v[164:167], a[104:107], a[200:203], v[164:167]\n"
    "s_waitcnt vmcnt(15)\n"
    "v_lshlrev_b32 v244, 16, v252\n"
    "v_and_b32 v245, s73, v252\n"
    "v_mfma_f32_16x16x32_bf16 v[168:171], a[104:107], a[208:211], v[168:171]\n"
    "s_waitcnt lgkmcnt(0)\n"
    "buffer_store_dwordx4 v[240:243], v231, s[44:47], s28 offen offset:128 sc1\n"
    "v_mfma_f32_16x16x32_bf16 v[168:171], a[108:111], a[212:215], v[168:171]\n"
    "v_lshlrev_b32 v246, 16, v253\n"
    "v_and_b32 v247, s73, v253\n"
    "v_mfma_f32_16x16x32_bf16 v[172:175], a[108:111], a[220:223], v[172:175]\n"
    "v_mfma_f32_16x16x32_bf16 v[172:175], a[104:107], a[216:219], v[172:175]\n"
    "v_lshlrev_b32 v248, 16, v254\n"
    "v_mfma_f32_16x16x32_bf16 v[176:179], a[112:115], a[160:163], v[176:179]\n"
    "buffer_load_dwordx4 a[104:107], v224, s[36:39], s60 offen offset:2048 sc1\n"
    "v_mfma_f32_16x16x32_bf16 v[176:179], a[116:119], a[164:167], v[176:179]\n"
    "buffer_load_dwordx4 a[108:111], v224, s[36:39], s60 offen offset:3072 sc1\n"
    "v_mfma_f32_16x16x32_bf16 v[180:183], a[116:119], a[172:175], v[180:183]\n"
    "s_add_u32 s60, s60, 4352\n"
    "v_and_b32 v249, s73, v254\n"
    "v_mfma_f32_16x16x32_bf16 v[180:183], a[112:115], a[168:171], v[180:183]\n"
    "v_lshlrev_b32 v250, 16, v255\n"
    "v_and_b32 v251, s73, v255\n"
    "v_mfma_f32_16x16x32_bf16 v[184:187], a[112:115], a[176:179], v[184:187]\n"
    "v_add_f32 v2, v244, v2\n"
    "v_add_f32 v34, v245, v34\n"
    "v_mfma_f32_16x16x32_bf16 v[184:187], a[116:119], a[180:183], v[184:187]\n"
    "v_mfma_f32_16x16x32_bf16 v[188:191], a[116:119], a[188:191], v[188:191]\n"
    "ds_read_b128 v[236:239], v232 offset:13312\n"
    "v_add_f32 v66, v246, v66\n"
    "v_mfma_f32_16x16x32_bf16 v[188:191], a[112:115], a[184:187], v[188:191]\n"
    "v_accvgpr_read_b32 v229, a226\n"
    "v_add_f32 v229, v247, v229\n"
    "v_mfma_f32_16x16x32_bf16 v[192:195], a[112:115], a[192:195], v[192:195]\n"
    "v_accvgpr_write_b32 a226, v229\n"
    "v_add_f32 v3, v248, v3\n"
    "v_mfma_f32_16x16x32_bf16 v[192:195], a[116:119], a[196:199], v[192:195]\n"
    "v_mfma_f32_16x16x32_bf16 v[196:199], a[116:119], a[204:207], v[196:199]\n"
    "buffer_load_dword v233, v224, s[48:51], 0 offen offset:64\n"
    "v_mfma_f32_16x16x32_bf16 v[196:199], a[112:115], a[200:203], v[196:199]\n"
    "v_add_f32 v35, v249, v35\n"
    "v_add_f32 v67, v250, v67\n"
    "v_mfma_f32_16x16x32_bf16 v[200:203], a[112:115], a[208:211], v[200:203]\n"
    "v_accvgpr_read_b32 v229, a227\n"
    "v_add_f32 v229, v251, v229\n"
    "v_mfma_f32_16x16x32_bf16 v[200:203], a[116:119], a[212:215], v[200:203]\n"
    "v_mfma_f32_16x16x32_bf16 v[204:207], a[116:119], a[220:223], v[204:207]\n"
    "v_accvgpr_write_b32 a227, v229\n"
    "v_add_f32 v6, v244, v6\n"
    "v_mfma_f32_16x16x32_bf16 v[204:207], a[112:115], a[216:219], v[204:207]\n"
    "buffer_load_dwordx4 a[112:115], v224, s[36:39], s60 offen offset:0 sc1\n"
    "v_mfma_f32_16x16x32_bf16 a[240:243], a[120:123], a[160:163], a[240:243]\n"
    "ds_read_b128 a[160:163], v226 offset:49152\n"
    "buffer_load_dwordx4 a[116:119], v224, s[36:39], s60 offen offset:1024 sc1\n"
    "v_mfma_f32_16x16x32_bf16 a[240:243], a[124:127], a[164:167], a[240:243]\n"
    "ds_read_b128 a[164:167], v227 offset:49152\n"
    "v_add_f32 v38, v245, v38\n"
    "v_mfma_f32_16x16x32_bf16 a[244:247], a[124:127], a[172:175], a[244:247]\n"
    "ds_read_b128 a[172:175], v227 offset:51200\n"
    "v_add_f32 v70, v246, v70\n"
    "v_mfma_f32_16x16x32_bf16 a[244:247], a[120:123], a[168:171], a[244:247]\n"
    "ds_read_b128 a[168:171], v226 offset:51200\n"
    "v_accvgpr_read_b32 v229, a230\n"
    "v_mfma_f32_16x16x32_bf16 a[248:251], a[120:123], a[176:179], a[248:251]\n"
    "ds_read_b128 a[176:179], v226 offset:53248\n"
    "v_mfma_f32_16x16x32_bf16 a[248:251], a[124:127], a[180:183], a[248:251]\n"
    "ds_read_b128 a[180:183], v227 offset:53248\n"
    "v_add_f32 v229, v247, v229\n"
    "v_mfma_f32_16x16x32_bf16 a[252:255], a[124:127], a[188:191], a[252:255]\n"
    "ds_read_b128 a[188:191], v227 offset:55296\n"
    "v_accvgpr_write_b32 a230, v229\n"
    "v_mfma_f32_16x16x32_bf16 a[252:255], a[120:123], a[184:187], a[252:255]\n"
    "ds_read_b128 a[184:187], v226 offset:55296\n"
    "v_add_f32 v7, v248, v7\n"
    "v_mfma_f32_16x16x32_bf16 v[208:211], a[120:123], a[192:195], v[208:211]\n"
    "ds_read_b128 a[192:195], v226 offset:57344\n"
    "v_mfma_f32_16x16x32_bf16 v[208:211], a[124:127], a[196:199], v[208:211]\n"
    "ds_read_b128 a[196:199], v227 offset:57344\n"
    "v_add_f32 v39, v249, v39\n"
    "v_mfma_f32_16x16x32_bf16 v[212:215], a[124:127], a[204:207], v[212:215]\n"
    "ds_read_b128 a[204:207], v227 offset:59392\n"
    "v_add_f32 v71, v250, v71\n"
    "v_mfma_f32_16x16x32_bf16 v[212:215], a[120:123], a[200:203], v[212:215]\n"
    "ds_read_b128 a[200:203], v226 offset:59392\n"
    "v_accvgpr_read_b32 v229, a231\n"
    "v_mfma_f32_16x16x32_bf16 v[216:219], a[120:123], a[208:211], v[216:219]\n"
    "ds_read_b128 a[208:211], v226 offset:61440\n"
    "v_mfma_f32_16x16x32_bf16 v[216:219], a[124:127], a[212:215], v[216:219]\n"
    "ds_read_b128 a[212:215], v227 offset:61440\n"
    "v_add_f32 v229, v251, v229\n"
    "v_mfma_f32_16x16x32_bf16 v[220:223], a[124:127], a[220:223], v[220:223]\n"
    "ds_read_b128 a[220:223], v227 offset:63488\n"
    "v_accvgpr_write_b32 a231, v229\n"
    "v_mfma_f32_16x16x32_bf16 v[220:223], a[120:123], a[216:219], v[220:223]\n"
    "ds_read_b128 a[216:219], v226 offset:63488\n"
    "s_waitcnt vmcnt(18) lgkmcnt(8)\n"
    "v_mfma_f32_16x16x32_bf16 v[112:115], a[128:131], a[160:163], v[112:115]\n"
    "buffer_load_dwordx4 a[120:123], v224, s[36:39], s60 offen offset:2048 sc1\n"
    "v_mfma_f32_16x16x32_bf16 v[112:115], a[132:135], a[164:167], v[112:115]\n"
    "buffer_load_dwordx4 a[124:127], v224, s[36:39], s60 offen offset:3072 sc1\n"
    "v_mfma_f32_16x16x32_bf16 v[116:119], a[132:135], a[172:175], v[116:119]\n"
    "s_add_u32 s60, s60, 4352\n"
    "v_add_f32 v10, v244, v10\n"
    "v_mfma_f32_16x16x32_bf16 v[116:119], a[128:131], a[168:171], v[116:119]\n"
    "buffer_store_dwordx4 v[236:239], v231, s[44:47], s29 offen offset:128 sc1\n"
    "v_mfma_f32_16x16x32_bf16 v[120:123], a[128:131], a[176:179], v[120:123]\n"
    "ds_read_b128 v[240:243], v232 offset:14336\n"
    "v_add_f32 v42, v245, v42\n"
    "v_mfma_f32_16x16x32_bf16 v[120:123], a[132:135], a[180:183], v[120:123]\n"
    "v_add_f32 v74, v246, v74\n"
    "v_accvgpr_read_b32 v229, a234\n"
    "v_mfma_f32_16x16x32_bf16 v[124:127], a[132:135], a[188:191], v[124:127]\n"
    "v_add_f32 v229, v247, v229\n"
    "v_accvgpr_write_b32 a234, v229\n"
    "v_mfma_f32_16x16x32_bf16 v[124:127], a[128:131], a[184:187], v[124:127]\n"
    "s_waitcnt lgkmcnt(1)\n"
    "v_mfma_f32_16x16x32_bf16 v[128:131], a[128:131], a[192:195], v[128:131]\n"
    "buffer_load_dword v233, v224, s[48:51], 0 offen offset:64\n"
    "v_mfma_f32_16x16x32_bf16 v[128:131], a[132:135], a[196:199], v[128:131]\n"
    "v_add_f32 v11, v248, v11\n"
    "v_add_f32 v43, v249, v43\n"
    "v_mfma_f32_16x16x32_bf16 v[132:135], a[132:135], a[204:207], v[132:135]\n"
    "v_add_f32 v75, v250, v75\n"
    "v_accvgpr_read_b32 v229, a235\n"
    "v_mfma_f32_16x16x32_bf16 v[132:135], a[128:131], a[200:203], v[132:135]\n"
    "v_add_u32 v226, 0x10000, v225\n"
    "v_xor_b32 v227, 64, v226\n"
    "v_mfma_f32_16x16x32_bf16 v[136:139], a[128:131], a[208:211], v[136:139]\n"
    "v_add_f32 v229, v251, v229\n"
    "v_accvgpr_write_b32 a235, v229\n"
    "v_mfma_f32_16x16x32_bf16 v[136:139], a[132:135], a[212:215], v[136:139]\n"
    "v_add_f32 v14, v244, v14\n"
    "v_add_f32 v46, v245, v46\n"
    "v_mfma_f32_16x16x32_bf16 v[140:143], a[132:135], a[220:223], v[140:143]\n"
    "s_waitcnt lgkmcnt(0)\n"
    "buffer_store_dwordx4 v[240:243], v231, s[44:47], s30 offen offset:128 sc1\n"
    "v_mfma_f32_16x16x32_bf16 v[140:143], a[128:131], a[216:219], v[140:143]\n"
    "s_waitcnt vmcnt(20)\n"
    "v_mfma_f32_16x16x32_bf16 v[144:147], a[136:139], a[160:163], v[144:147]\n"
    "buffer_load_dwordx4 a[128:131], v224, s[36:39], s60 offen offset:0 sc1\n"
    "v_mfma_f32_16x16x32_bf16 v[144:147], a[140:143], a[164:167], v[144:147]\n"
    "buffer_load_dwordx4 a[132:135], v224, s[36:39], s60 offen offset:1024 sc1\n"
    "v_mfma_f32_16x16x32_bf16 v[148:151], a[140:143], a[172:175], v[148:151]\n"
    "v_mfma_f32_16x16x32_bf16 v[148:151], a[136:139], a[168:171], v[148:151]\n"
    "v_add_f32 v78, v246, v78\n"
    "v_accvgpr_read_b32 v229, a238\n"
    "v_mfma_f32_16x16x32_bf16 v[152:155], a[136:139], a[176:179], v[152:155]\n"
    "ds_read_b128 v[236:239], v232 offset:15360\n"
    "v_add_f32 v229, v247, v229\n"
    "v_mfma_f32_16x16x32_bf16 v[152:155], a[140:143], a[180:183], v[152:155]\n"
    "v_accvgpr_write_b32 a238, v229\n"
    "v_add_f32 v15, v248, v15\n"
    "v_mfma_f32_16x16x32_bf16 v[156:159], a[140:143], a[188:191], v[156:159]\n"
    "v_mfma_f32_16x16x32_bf16 v[156:159], a[136:139], a[184:187], v[156:159]\n"
    "v_add_f32 v47, v249, v47\n"
    "v_add_f32 v79, v250, v79\n"
    "v_mfma_f32_16x16x32_bf16 v[160:163], a[136:139], a[192:195], v[160:163]\n"
    "v_accvgpr_read_b32 v229, a239\n"
    "v_add_f32 v229, v251, v229\n"
    "v_mfma_f32_16x16x32_bf16 v[160:163], a[140:143], a[196:199], v[160:163]\n"
    "v_accvgpr_write_b32 a239, v229\n"
    "v_add_f32 v18, v244, v18\n"
    "v_mfma_f32_16x16x32_bf16 v[164:167], a[140:143], a[204:207], v[164:167]\n"
    "v_mfma_f32_16x16x32_bf16 v[164:167], a[136:139], a[200:203], v[164:167]\n"
    "v_add_f32 v50, v245, v50\n"
    "v_add_f32 v82, v246, v82\n"
    "v_mfma_f32_16x16x32_bf16 v[168:171], a[136:139], a[208:211], v[168:171]\n"
    "v_add_f32 v98, v247, v98\n"
    "v_add_f32 v19, v248, v19\n"
    "v_mfma_f32_16x16x32_bf16 v[168:171], a[140:143], a[212:215], v[168:171]\n"
    "v_add_f32 v51, v249, v51\n"
    "v_add_f32 v83, v250, v83\n"
    "v_mfma_f32_16x16x32_bf16 v[172:175], a[140:143], a[220:223], v[172:175]\n"
    "s_waitcnt lgkmcnt(0)\n"
    "buffer_store_dwordx4 v[236:239], v231, s[44:47], s31 offen offset:128 sc1\n"
    "v_mfma_f32_16x16x32_bf16 v[172:175], a[136:139], a[216:219], v[172:175]\n"
    "v_add_f32 v99, v251, v99\n"
    "s_waitcnt vmcnt(20)\n"
    "v_mfma_f32_16x16x32_bf16 v[176:179], a[144:147], a[160:163], v[176:179]\n"
    "buffer_load_dwordx4 a[136:139], v224, s[36:39], s60 offen offset:2048 sc1\n"
    "v_mfma_f32_16x16x32_bf16 v[176:179], a[148:151], a[164:167], v[176:179]\n"
    "buffer_load_dwordx4 a[140:143], v224, s[36:39], s60 offen offset:3072 sc1\n"
    "v_mfma_f32_16x16x32_bf16 v[180:183], a[148:151], a[172:175], v[180:183]\n"
    "s_add_u32 s60, s60, 4352\n"
    "v_add_f32 v22, v244, v22\n"
    "v_mfma_f32_16x16x32_bf16 v[180:183], a[144:147], a[168:171], v[180:183]\n"
    "v_add_f32 v54, v245, v54\n"
    "v_add_f32 v86, v246, v86\n"
    "v_mfma_f32_16x16x32_bf16 v[184:187], a[144:147], a[176:179], v[184:187]\n"
    "v_add_f32 v102, v247, v102\n"
    "v_add_f32 v23, v248, v23\n"
    "v_mfma_f32_16x16x32_bf16 v[184:187], a[148:151], a[180:183], v[184:187]\n"
    "v_mfma_f32_16x16x32_bf16 v[188:191], a[148:151], a[188:191], v[188:191]\n"
    "v_add_f32 v55, v249, v55\n"
    "v_add_f32 v87, v250, v87\n"
    "v_mfma_f32_16x16x32_bf16 v[188:191], a[144:147], a[184:187], v[188:191]\n"
    "v_add_f32 v103, v251, v103\n"
    "v_add_f32 v26, v244, v26\n"
    "v_mfma_f32_16x16x32_bf16 v[192:195], a[144:147], a[192:195], v[192:195]\n"
    "v_add_f32 v58, v245, v58\n"
    "v_add_f32 v90, v246, v90\n"
    "v_mfma_f32_16x16x32_bf16 v[192:195], a[148:151], a[196:199], v[192:195]\n"
    "v_mfma_f32_16x16x32_bf16 v[196:199], a[148:151], a[204:207], v[196:199]\n"
    "v_add_f32 v106, v247, v106\n"
    "v_add_f32 v27, v248, v27\n"
    "v_mfma_f32_16x16x32_bf16 v[196:199], a[144:147], a[200:203], v[196:199]\n"
    "v_add_f32 v59, v249, v59\n"
    "v_add_f32 v91, v250, v91\n"
    "v_mfma_f32_16x16x32_bf16 v[200:203], a[144:147], a[208:211], v[200:203]\n"
    "v_add_f32 v107, v251, v107\n"
    "v_add_f32 v30, v244, v30\n"
    "v_mfma_f32_16x16x32_bf16 v[200:203], a[148:151], a[212:215], v[200:203]\n"
    "v_mfma_f32_16x16x32_bf16 v[204:207], a[148:151], a[220:223], v[204:207]\n"
    "s_barrier\n"
    "v_mfma_f32_16x16x32_bf16 v[204:207], a[144:147], a[216:219], v[204:207]\n"
    "buffer_load_dwordx4 a[144:147], v224, s[36:39], s60 offen offset:0 sc1\n"
    "s_waitcnt vmcnt(21)\n"
    "v_mfma_f32_16x16x32_bf16 a[240:243], a[152:155], a[160:163], a[240:243]\n"
    "ds_read_b128 a[160:163], v226 offset:0\n"
    "buffer_load_dwordx4 a[148:151], v224, s[36:39], s60 offen offset:1024 sc1\n"
    "v_mfma_f32_16x16x32_bf16 a[240:243], a[156:159], a[164:167], a[240:243]\n"
    "ds_read_b128 a[164:167], v227 offset:0\n"
    "v_add_f32 v62, v245, v62\n"
    "v_mfma_f32_16x16x32_bf16 a[244:247], a[156:159], a[172:175], a[244:247]\n"
    "ds_read_b128 a[172:175], v227 offset:2048\n"
    "v_add_f32 v94, v246, v94\n"
    "v_mfma_f32_16x16x32_bf16 a[244:247], a[152:155], a[168:171], a[244:247]\n"
    "ds_read_b128 a[168:171], v226 offset:2048\n"
    "v_add_f32 v110, v247, v110\n"
    "v_mfma_f32_16x16x32_bf16 a[248:251], a[152:155], a[176:179], a[248:251]\n"
    "ds_read_b128 a[176:179], v226 offset:4096\n"
    "v_mfma_f32_16x16x32_bf16 a[248:251], a[156:159], a[180:183], a[248:251]\n"
    "ds_read_b128 a[180:183], v227 offset:4096\n"
    "v_add_f32 v31, v248, v31\n"
    "v_mfma_f32_16x16x32_bf16 a[252:255], a[156:159], a[188:191], a[252:255]\n"
    "ds_read_b128 a[188:191], v227 offset:6144\n"
    "v_add_f32 v63, v249, v63\n"
    "v_mfma_f32_16x16x32_bf16 a[252:255], a[152:155], a[184:187], a[252:255]\n"
    "ds_read_b128 a[184:187], v226 offset:6144\n"
    "v_add_f32 v95, v250, v95\n"
    "v_mfma_f32_16x16x32_bf16 v[208:211], a[152:155], a[192:195], v[208:211]\n"
    "ds_read_b128 a[192:195], v226 offset:8192\n"
    "v_mfma_f32_16x16x32_bf16 v[208:211], a[156:159], a[196:199], v[208:211]\n"
    "ds_read_b128 a[196:199], v227 offset:8192\n"
    "v_add_f32 v111, v251, v111\n"
    "v_mfma_f32_16x16x32_bf16 v[212:215], a[156:159], a[204:207], v[212:215]\n"
    "ds_read_b128 a[204:207], v227 offset:10240\n"
    "v_mfma_f32_16x16x32_bf16 v[212:215], a[152:155], a[200:203], v[212:215]\n"
    "ds_read_b128 a[200:203], v226 offset:10240\n"
    "v_mfma_f32_16x16x32_bf16 v[216:219], a[152:155], a[208:211], v[216:219]\n"
    "ds_read_b128 a[208:211], v226 offset:12288\n"
    "v_mfma_f32_16x16x32_bf16 v[216:219], a[156:159], a[212:215], v[216:219]\n"
    "ds_read_b128 a[212:215], v227 offset:12288\n"
    "v_mfma_f32_16x16x32_bf16 v[220:223], a[156:159], a[220:223], v[220:223]\n"
    "ds_read_b128 a[220:223], v227 offset:14336\n"
    "v_mfma_f32_16x16x32_bf16 v[220:223], a[152:155], a[216:219], v[220:223]\n"
    "ds_read_b128 a[216:219], v226 offset:14336\n"
    "L_MB_%=:\n"
    "s_waitcnt vmcnt(18) lgkmcnt(8)\n"
    "v_mfma_f32_16x16x32_bf16 v[0:3], a[96:99], a[160:163], v[0:3]\n"
    "buffer_load_dwordx4 a[152:155], v224, s[36:39], s60 offen offset:2048 sc1\n"
    "v_mfma_f32_16x16x32_bf16 v[0:3], a[100:103], a[164:167], v[0:3]\n"
    "buffer_load_dwordx4 a[156:159], v224, s[36:39], s60 offen offset:3072 sc1\n"
    "v_mfma_f32_16x16x32_bf16 v[4:7], a[100:103], a[172:175], v[4:7]\n"
    "s_add_u32 s60, s60, 4352\n"
    "v_mfma_f32_16x16x32_bf16 v[4:7], a[96:99], a[168:171], v[4:7]\n"
    "v_mfma_f32_16x16x32_bf16 v[8:11], a[96:99], a[176:179], v[8:11]\n"
    "v_mfma_f32_16x16x32_bf16 v[8:11], a[100:103], a[180:183], v[8:11]\n"
    "v_mfma_f32_16x16x32_bf16 v[12:15], a[100:103], a[188:191], v[12:15]\n"
    "buffer_load_dword v233, v224, s[52:55], 0 offen sc1\n"
    "v_mfma_f32_16x16x32_bf16 v[12:15], a[96:99], a[184:187], v[12:15]\n"
    "s_waitcnt lgkmcnt(0)\n"
    "v_mfma_f32_16x16x32_bf16 v[16:19], a[96:99], a[192:195], v[16:19]\n"
    "v_mfma_f32_16x16x32_bf16 v[16:19], a[100:103], a[196:199], v[16:19]\n"
    "v_mfma_f32_16x16x32_bf16 v[20:23], a[100:103], a[204:207], v[20:23]\n"
    "v_mfma_f32_16x16x32_bf16 v[20:23], a[96:99], a[200:203], v[20:23]\n"
    "v_mfma_f32_16x16x32_bf16 v[24:27], a[96:99], a[208:211], v[24:27]\n"
    "v_mfma_f32_16x16x32_bf16 v[24:27], a[100:103], a[212:215], v[24:27]\n"
    "v_mfma_f32_16x16x32_bf16 v[28:31], a[100:103], a[220:223], v[28:31]\n"
    "v_mfma_f32_16x16x32_bf16 v[28:31], a[96:99], a[216:219], v[28:31]\n"
    "s_waitcnt vmcnt(18)\n"
    "v_mfma_f32_16x16x32_bf16 v[32:35], a[104:107], a[160:163], v[32:35]\n"
    "buffer_load_dwordx4 a[96:99], v224, s[36:39], s60 offen offset:0 sc1\n"
    "v_mfma_f32_16x16x32_bf16 v[32:35], a[108:111], a[164:167], v[32:35]\n"
    "buffer_load_dwordx4 a[100:103], v224, s[36:39], s60 offen offset:1024 sc1\n"
    "v_mfma_f32_16x16x32_bf16 v[36:39], a[108:111], a[172:175], v[36:39]\n"
    "v_mfma_f32_16x16x32_bf16 v[36:39], a[104:107], a[168:171], v[36:39]\n"
    "v_mfma_f32_16x16x32_bf16 v[40:43], a[104:107], a[176:179], v[40:43]\n"
    "v_mfma_f32_16x16x32_bf16 v[40:43], a[108:111], a[180:183], v[40:43]\n"
    "v_mfma_f32_16x16x32_bf16 v[44:47], a[108:111], a[188:191], v[44:47]\n"
    "v_mfma_f32_16x16x32_bf16 v[44:47], a[104:107], a[184:187], v[44:47]\n"
    "v_mfma_f32_16x16x32_bf16 v[48:51], a[104:107], a[192:195], v[48:51]\n"
    "v_mfma_f32_16x16x32_bf16 v[48:51], a[108:111], a[196:199], v[48:51]\n"
    "v_mfma_f32_16x16x32_bf16 v[52:55], a[108:111], a[204:207], v[52:55]\n"
    "v_mfma_f32_16x16x32_bf16 v[52:55], a[104:107], a[200:203], v[52:55]\n"
    "v_mfma_f32_16x16x32_bf16 v[56:59], a[104:107], a[208:211], v[56:59]\n"
    "v_mfma_f32_16x16x32_bf16 v[56:59], a[108:111], a[212:215], v[56:59]\n"
    "v_mfma_f32_16x16x32_bf16 v[60:63], a[108:111], a[220:223], v[60:63]\n"
    "v_mfma_f32_16x16x32_bf16 v[60:63], a[104:107], a[216:219], v[60:63]\n"
    "s_waitcnt vmcnt(17)\n"
    "v_mfma_f32_16x16x32_bf16 v[64:67], a[112:115], a[160:163], v[64:67]\n"
    "buffer_load_dwordx4 a[104:107], v224, s[36:39], s60 offen offset:2048 sc1\n"
    "v_mfma_f32_16x16x32_bf16 v[64:67], a[116:119], a[164:167], v[64:67]\n"
    "buffer_load_dwordx4 a[108:111], v224, s[36:39], s60 offen offset:3072 sc1\n"
    "v_mfma_f32_16x16x32_bf16 v[68:71], a[116:119], a[172:175], v[68:71]\n"
    "s_add_u32 s60, s60, 4352\n"
    "v_mfma_f32_16x16x32_bf16 v[68:71], a[112:115], a[168:171], v[68:71]\n"
    "v_mfma_f32_16x16x32_bf16 v[72:75], a[112:115], a[176:179], v[72:75]\n"
    "v_mfma_f32_16x16x32_bf16 v[72:75], a[116:119], a[180:183], v[72:75]\n"
    "v_mfma_f32_16x16x32_bf16 v[76:79], a[116:119], a[188:191], v[76:79]\n"
    "v_mfma_f32_16x16x32_bf16 v[76:79], a[112:115], a[184:187], v[76:79]\n"
    "v_mfma_f32_16x16x32_bf16 v[80:83], a[112:115], a[192:195], v[80:83]\n"
    "v_mfma_f32_16x16x32_bf16 v[80:83], a[116:119], a[196:199], v[80:83]\n"
    "v_mfma_f32_16x16x32_bf16 v[84:87], a[116:119], a[204:207], v[84:87]\n"
    "v_mfma_f32_16x16x32_bf16 v[84:87], a[112:115], a[200:203], v[84:87]\n"
    "v_mfma_f32_16x16x32_bf16 v[88:91], a[112:115], a[208:211], v[88:91]\n"
    "v_mfma_f32_16x16x32_bf16 v[88:91], a[116:119], a[212:215], v[88:91]\n"
    "v_mfma_f32_16x16x32_bf16 v[92:95], a[116:119], a[220:223], v[92:95]\n"
    "v_mfma_f32_16x16x32_bf16 v[92:95], a[112:115], a[216:219], v[92:95]\n"
    "s_waitcnt vmcnt(16)\n"
    "v_mfma_f32_16x16x32_bf16 a[224:227], a[120:123], a[160:163], a[224:227]\n"
    "buffer_load_dwordx4 a[112:115], v224, s[36:39], s60 offen offset:0 sc1\n"
    "v_mfma_f32_16x16x32_bf16 a[224:227], a[124:127], a[164:167], a[224:227]\n"
    "buffer_load_dwordx4 a[116:119], v224, s[36:39], s60 offen offset:1024 sc1\n"
    "v_mfma_f32_16x16x32_bf16 a[228:231], a[124:127], a[172:175], a[228:231]\n"
    "v_mfma_f32_16x16x32_bf16 a[228:231], a[120:123], a[168:171], a[228:231]\n"
    "v_mfma_f32_16x16x32_bf16 a[232:235], a[120:123], a[176:179], a[232:235]\n"
    "v_mfma_f32_16x16x32_bf16 a[232:235], a[124:127], a[180:183], a[232:235]\n"
    "v_mfma_f32_16x16x32_bf16 a[236:239], a[124:127], a[188:191], a[236:239]\n"
    "v_mfma_f32_16x16x32_bf16 a[236:239], a[120:123], a[184:187], a[236:239]\n"
    "v_mfma_f32_16x16x32_bf16 v[96:99], a[120:123], a[192:195], v[96:99]\n"
    "v_mfma_f32_16x16x32_bf16 v[96:99], a[124:127], a[196:199], v[96:99]\n"
    "v_mfma_f32_16x16x32_bf16 v[100:103], a[124:127], a[204:207], v[100:103]\n"
    "v_mfma_f32_16x16x32_bf16 v[100:103], a[120:123], a[200:203], v[100:103]\n"
    "v_mfma_f32_16x16x32_bf16 v[104:107], a[120:123], a[208:211], v[104:107]\n"
    "v_mfma_f32_16x16x32_bf16 v[104:107], a[124:127], a[212:215], v[104:107]\n"
    "v_mfma_f32_16x16x32_bf16 v[108:111], a[124:127], a[220:223], v[108:111]\n"
    "v_mfma_f32_16x16x32_bf16 v[108:111], a[120:123], a[216:219], v[108:111]\n"
    "s_waitcnt vmcnt(14)\n"
    "v_mfma_f32_16x16x32_bf16 v[112:115], a[128:131], a[160:163], v[112:115]\n"
    "buffer_load_dwordx4 a[120:123], v224, s[36:39], s60 offen offset:2048 sc1\n"
    "v_mfma_f32_16x16x32_bf16 v[112:115], a[132:135], a[164:167], v[112:115]\n"
    "buffer_load_dwordx4 a[124:127], v224, s[36:39], s60 offen offset:3072 sc1\n"
    "v_mfma_f32_16x16x32_bf16 v[116:119], a[132:135], a[172:175], v[116:119]\n"
    "s_add_u32 s60, s60, 4352\n"
    "v_mfma_f32_16x16x32_bf16 v[116:119], a[128:131], a[168:171], v[116:119]\n"
    "v_mfma_f32_16x16x32_bf16 v[120:123], a[128:131], a[176:179], v[120:123]\n"
    "v_mfma_f32_16x16x32_bf16 v[120:123], a[132:135], a[180:183], v[120:123]\n"
    "v_mfma_f32_16x16x32_bf16 v[124:127], a[132:135], a[188:191], v[124:127]\n"
    "v_mfma_f32_16x16x32_bf16 v[124:127], a[128:131], a[184:187], v[124:127]\n"
    "v_mfma_f32_16x16x32_bf16 v[128:131], a[128:131], a[192:195], v[128:131]\n"
    "v_mfma_f32_16x16x32_bf16 v[128:131], a[132:135], a[196:199], v[128:131]\n"
    "s_and_b32 s65, s64, 15\n"
    "s_lshl_b32 s65, s65, 7\n"
    "v_mfma_f32_16x16x32_bf16 v[132:135], a[132:135], a[204:207], v[132:135]\n"
    "v_mfma_f32_16x16x32_bf16 v[132:135], a[128:131], a[200:203], v[132:135]\n"
    "v_mfma_f32_16x16x32_bf16 v[136:139], a[128:131], a[208:211], v[136:139]\n"
    "v_mfma_f32_16x16x32_bf16 v[136:139], a[132:135], a[212:215], v[136:139]\n"
    "v_mfma_f32_16x16x32_bf16 v[140:143], a[132:135], a[220:223], v[140:143]\n"
    "v_mfma_f32_16x16x32_bf16 v[140:143], a[128:131], a[216:219], v[140:143]\n"
    "s_waitcnt vmcnt(13)\n"
    "v_mfma_f32_16x16x32_bf16 v[144:147], a[136:139], a[160:163], v[144:147]\n"
    "buffer_load_dwordx4 a[128:131], v224, s[36:39], s60 offen offset:0 sc1\n"
    "v_mfma_f32_16x16x32_bf16 v[144:147], a[140:143], a[164:167], v[144:147]\n"
    "buffer_load_dwordx4 a[132:135], v224, s[36:39], s60 offen offset:1024 sc1\n"
    "v_mfma_f32_16x16x32_bf16 v[148:151], a[140:143], a[172:175], v[148:151]\n"
    "s_add_u32 s65, s65, s66\n"
    "s_lshr_b32 s64, s64, 4\n"
    "v_mfma_f32_16x16x32_bf16 v[148:151], a[136:139], a[168:171], v[148:151]\n"
    "v_mfma_f32_16x16x32_bf16 v[152:155], a[136:139], a[176:179], v[152:155]\n"
    "v_mfma_f32_16x16x32_bf16 v[152:155], a[140:143], a[180:183], v[152:155]\n"
    "v_mfma_f32_16x16x32_bf16 v[156:159], a[140:143], a[188:191], v[156:159]\n"
    "v_mfma_f32_16x16x32_bf16 v[156:159], a[136:139], a[184:187], v[156:159]\n"
    "v_mfma_f32_16x16x32_bf16 v[160:163], a[136:139], a[192:195], v[160:163]\n"
    "v_mfma_f32_16x16x32_bf16 v[160:163], a[140:143], a[196:199], v[160:163]\n"
    "v_mfma_f32_16x16x32_bf16 v[164:167], a[140:143], a[204:207], v[164:167]\n"
    "v_mfma_f32_16x16x32_bf16 v[164:167], a[136:139], a[200:203], v[164:167]\n"
    "s_add_u32 m0, s67, s85\n"
    "s_add_u32 s67, s67, 32768\n"
    "v_mfma_f32_16x16x32_bf16 v[168:171], a[136:139], a[208:211], v[168:171]\n"
    "v_mfma_f32_16x16x32_bf16 v[168:171], a[140:143], a[212:215], v[168:171]\n"
    "v_mfma_f32_16x16x32_bf16 v[172:175], a[140:143], a[220:223], v[172:175]\n"
    "v_mfma_f32_16x16x32_bf16 v[172:175], a[136:139], a[216:219], v[172:175]\n"
    "s_waitcnt vmcnt(13)\n"
    "v_mfma_f32_16x16x32_bf16 v[176:179], a[144:147], a[160:163], v[176:179]\n"
    "buffer_load_dwordx4 a[136:139], v224, s[36:39], s60 offen offset:2048 sc1\n"
    "v_mfma_f32_16x16x32_bf16 v[176:179], a[148:151], a[164:167], v[176:179]\n"
    "buffer_load_dwordx4 a[140:143], v224, s[36:39], s60 offen offset:3072 sc1\n"
    "v_mfma_f32_16x16x32_bf16 v[180:183], a[148:151], a[172:175], v[180:183]\n"
    "s_add_u32 s60, s60, 4352\n"
    "v_mfma_f32_16x16x32_bf16 v[180:183], a[144:147], a[168:171], v[180:183]\n"
    "v_mfma_f32_16x16x32_bf16 v[184:187], a[144:147], a[176:179], v[184:187]\n"
    "s_and_b32 s67, s67, 0x1ffff\n"
    "v_mfma_f32_16x16x32_bf16 v[184:187], a[148:151], a[180:183], v[184:187]\n"
    "v_mfma_f32_16x16x32_bf16 v[188:191], a[148:151], a[188:191], v[188:191]\n"
    "v_mfma_f32_16x16x32_bf16 v[188:191], a[144:147], a[184:187], v[188:191]\n"
    "v_mfma_f32_16x16x32_bf16 v[192:195], a[144:147], a[192:195], v[192:195]\n"
    "v_mfma_f32_16x16x32_bf16 v[192:195], a[148:151], a[196:199], v[192:195]\n"
    "v_mfma_f32_16x16x32_bf16 v[196:199], a[148:151], a[204:207], v[196:199]\n"
    "v_mfma_f32_16x16x32_bf16 v[196:199], a[144:147], a[200:203], v[196:199]\n"
    "v_mov_b32 v228, s80\n"
    "v_mfma_f32_16x16x32_bf16 v[200:203], a[144:147], a[208:211], v[200:203]\n"
    "v_mfma_f32_16x16x32_bf16 v[200:203], a[148:151], a[212:215], v[200:203]\n"
    "v_mfma_f32_16x16x32_bf16 v[204:207], a[148:151], a[220:223], v[204:207]\n"
    "buffer_store_dword v228, v224, s[48:51], 0 offen sc1\n"
    "v_mfma_f32_16x16x32_bf16 v[204:207], a[144:147], a[216:219], v[204:207]\n"
    "buffer_load_dwordx4 a[144:147], v224, s[36:39], s60 offen offset:0 sc1\n"
    "s_waitcnt vmcnt(15)\n"
    "v_mfma_f32_16x16x32_bf16 a[240:243], a[152:155], a[160:163], a[240:243]\n"
    "ds_read_b128 a[160:163], v226 offset:16384\n"
    "buffer_load_dwordx4 a[148:151], v224, s[36:39], s60 offen offset:1024 sc1\n"
    "v_mfma_f32_16x16x32_bf16 a[240:243], a[156:159], a[164:167], a[240:243]\n"
    "ds_read_b128 a[164:167], v227 offset:16384\n"
    "v_mfma_f32_16x16x32_bf16 a[244:247], a[156:159], a[172:175], a[244:247]\n"
    "ds_read_b128 a[172:175], v227 offset:18432\n"
    "v_mfma_f32_16x16x32_bf16 a[244:247], a[152:155], a[168:171], a[244:247]\n"
    "ds_read_b128 a[168:171], v226 offset:18432\n"
    "v_mfma_f32_16x16x32_bf16 a[248:251], a[152:155], a[176:179], a[248:251]\n"
    "ds_read_b128 a[176:179], v226 offset:20480\n"
    "v_mfma_f32_16x16x32_bf16 a[248:251], a[156:159], a[180:183], a[248:251]\n"
    "ds_read_b128 a[180:183], v227 offset:20480\n"
    "v_mfma_f32_16x16x32_bf16 a[252:255], a[156:159], a[188:191], a[252:255]\n"
    "ds_read_b128 a[188:191], v227 offset:22528\n"
    "v_mfma_f32_16x16x32_bf16 a[252:255], a[152:155], a[184:187], a[252:255]\n"
    "ds_read_b128 a[184:187], v226 offset:22528\n"
    "v_mfma_f32_16x16x32_bf16 v[208:211], a[152:155], a[192:195], v[208:211]\n"
    "ds_read_b128 a[192:195], v226 offset:24576\n"
    "v_mfma_f32_16x16x32_bf16 v[208:211], a[156:159], a[196:199], v[208:211]\n"
    "ds_read_b128 a[196:199], v227 offset:24576\n"
    "v_mfma_f32_16x16x32_bf16 v[212:215], a[156:159], a[204:207], v[212:215]\n"
    "ds_read_b128 a[204:207], v227 offset:26624\n"
    "v_mfma_f32_16x16x32_bf16 v[212:215], a[152:155], a[200:203], v[212:215]\n"
    "ds_read_b128 a[200:203], v226 offset:26624\n"
    "v_mfma_f32_16x16x32_bf16 v[216:219], a[152:155], a[208:211], v[216:219]\n"
    "ds_read_b128 a[208:211], v226 offset:28672\n"
    "v_mfma_f32_16x16x32_bf16 v[216:219], a[156:159], a[212:215], v[216:219]\n"
    "ds_read_b128 a[212:215], v227 offset:28672\n"
    "v_mfma_f32_16x16x32_bf16 v[220:223], a[156:159], a[220:223], v[220:223]\n"
    "ds_read_b128 a[220:223], v227 offset:30720\n"
    "v_mfma_f32_16x16x32_bf16 v[220:223], a[152:155], a[216:219], v[220:223]\n"
    "ds_read_b128 a[216:219], v226 offset:30720\n"
    "s_waitcnt vmcnt(13) lgkmcnt(8)\n"
    "v_mfma_f32_16x16x32_bf16 v[0:3], a[96:99], a[160:163], v[0:3]\n"
    "buffer_load_dwordx4 a[152:155], v224, s[36:39], s60 offen offset:2048 sc1\n"
    "v_mfma_f32_16x16x32_bf16 v[0:3], a[100:103], a[164:167], v[0:3]\n"
    "buffer_load_dwordx4 a[156:159], v224, s[36:39], s60 offen offset:3072 sc1\n"
    "v_mfma_f32_16x16x32_bf16 v[4:7], a[100:103], a[172:175], v[4:7]\n"
    "s_add_u32 s60, s60, 4352\n"
    "v_mfma_f32_16x16x32_bf16 v[4:7], a[96:99], a[168:171], v[4:7]\n"
    "v_mfma_f32_16x16x32_bf16 v[8:11], a[96:99], a[176:179], v[8:11]\n"
    "v_mfma_f32_16x16x32_bf16 v[8:11], a[100:103], a[180:183], v[8:11]\n"
    "v_mfma_f32_16x16x32_bf16 v[12:15], a[100:103], a[188:191], v[12:15]\n"
    "v_cmp_le_u32 vcc, s78, v233\n"
    "s_and_b32 s81, vcc_lo, 15\n"
    "v_mfma_f32_16x16x32_bf16 v[12:15], a[96:99], a[184:187], v[12:15]\n"
    "s_waitcnt lgkmcnt(0)\n"
    "v_mfma_f32_16x16x32_bf16 v[16:19], a[96:99], a[192:195], v[16:19]\n"
    "v_mfma_f32_16x16x32_bf16 v[16:19], a[100:103], a[196:199], v[16:19]\n"
    "v_mfma_f32_16x16x32_bf16 v[20:23], a[100:103], a[204:207], v[20:23]\n"
    "s_cmp_eq_u32 s81, 15\n"
    "s_cbranch_scc0 L_SLOW_%=\n"
    "L_BACK_%=:\n"
    "v_mfma_f32_16x16x32_bf16 v[20:23], a[96:99], a[200:203], v[20:23]\n"
    "v_mfma_f32_16x16x32_bf16 v[24:27], a[96:99], a[208:211], v[24:27]\n"
    "v_mfma_f32_16x16x32_bf16 v[24:27], a[100:103], a[212:215], v[24:27]\n"
    "v_mfma_f32_16x16x32_bf16 v[28:31], a[100:103], a[220:223], v[28:31]\n"
    "v_mfma_f32_16x16x32_bf16 v[28:31], a[96:99], a[216:219], v[28:31]\n"
    "s_waitcnt vmcnt(13)\n"
    "v_mfma_f32_16x16x32_bf16 v[32:35], a[104:107], a[160:163], v[32:35]\n"
    "buffer_load_dwordx4 a[96:99], v224, s[36:39], s60 offen offset:0 sc1\n"
    "v_mfma_f32_16x16x32_bf16 v[32:35], a[108:111], a[164:167], v[32:35]\n"
    "buffer_load_dwordx4 a[100:103], v224, s[36:39], s60 offen offset:1024 sc1\n"
    "v_mfma_f32_16x16x32_bf16 v[36:39], a[108:111], a[172:175], v[36:39]\n"
    "v_mfma_f32_16x16x32_bf16 v[36:39], a[104:107], a[168:171], v[36:39]\n"
    "v_mfma_f32_16x16x32_bf16 v[40:43], a[104:107], a[176:179], v[40:43]\n"
    "buffer_load_dwordx4 v231, s[40:43], s65 offen sc1 lds\n"
    "v_mfma_f32_16x16x32_bf16 v[40:43], a[108:111], a[180:183], v[40:43]\n"
    "v_mfma_f32_16x16x32_bf16 v[44:47], a[108:111], a[188:191], v[44:47]\n"
    "v_mfma_f32_16x16x32_bf16 v[44:47], a[104:107], a[184:187], v[44:47]\n"
    "v_mfma_f32_16x16x32_bf16 v[48:51], a[104:107], a[192:195], v[48:51]\n"
    "s_add_u32 m0, m0, 1024\n"
    "s_add_u32 s65, s65, 16384\n"
    "v_mfma_f32_16x16x32_bf16 v[48:51], a[108:111], a[196:199], v[48:51]\n"
    "v_mfma_f32_16x16x32_bf16 v[52:55], a[108:111], a[204:207], v[52:55]\n"
    "v_mfma_f32_16x16x32_bf16 v[52:55], a[104:107], a[200:203], v[52:55]\n"
    "v_mfma_f32_16x16x32_bf16 v[56:59], a[104:107], a[208:211], v[56:59]\n"
    "v_mfma_f32_16x16x32_bf16 v[56:59], a[108:111], a[212:215], v[56:59]\n"
    "buffer_load_dwordx4 v231, s[40:43], s65 offen sc1 lds\n"
    "v_mfma_f32_16x16x32_bf16 v[60:63], a[108:111], a[220:223], v[60:63]\n"
    "v_mfma_f32_16x16x32_bf16 v[60:63], a[104:107], a[216:219], v[60:63]\n"
    "s_waitcnt vmcnt(15)\n"
    "v_mfma_f32_16x16x32_bf16 v[64:67], a[112:115], a[160:163], v[64:67]\n"
    "buffer_load_dwordx4 a[104:107], v224, s[36:39], s60 offen offset:2048 sc1\n"
    "v_mfma_f32_16x16x32_bf16 v[64:67], a[116:119], a[164:167], v[64:67]\n"
    "buffer_load_dwordx4 a[108:111], v224, s[36:39], s60 offen offset:3072 sc1\n"
    "v_mfma_f32_16x16x32_bf16 v[68:71], a[116:119], a[172:175], v[68:71]\n"
    "s_add_u32 s60, s60, 4352\n"
    "v_mfma_f32_16x16x32_bf16 v[68:71], a[112:115], a[168:171], v[68:71]\n"
    "v_mfma_f32_16x16x32_bf16 v[72:75], a[112:115], a[176:179], v[72:75]\n"
    "v_mfma_f32_16x16x32_bf16 v[72:75], a[116:119], a[180:183], v[72:75]\n"
    "s_add_u32 m0, m0, 1024\n"
    "s_add_u32 s65, s65, 16384\n"
    "v_mfma_f32_16x16x32_bf16 v[76:79], a[116:119], a[188:191], v[76:79]\n"
    "v_mfma_f32_16x16x32_bf16 v[76:79], a[112:115], a[184:187], v[76:79]\n"
    "v_mfma_f32_16x16x32_bf16 v[80:83], a[112:115], a[192:195], v[80:83]\n"
    "v_mfma_f32_16x16x32_bf16 v[80:83], a[116:119], a[196:199], v[80:83]\n"
    "buffer_load_dwordx4 v231, s[40:43], s65 offen sc1 lds\n"
    "v_mfma_f32_16x16x32_bf16 v[84:87], a[116:119], a[204:207], v[84:87]\n"
    "v_mfma_f32_16x16x32_bf16 v[84:87], a[112:115], a[200:203], v[84:87]\n"
    "v_mfma_f32_16x16x32_bf16 v[88:91], a[112:115], a[208:211], v[88:91]\n"
    "v_mfma_f32_16x16x32_bf16 v[88:91], a[116:119], a[212:215], v[88:91]\n"
    "v_mfma_f32_16x16x32_bf16 v[92:95], a[116:119], a[220:223], v[92:95]\n"
    "s_add_u32 m0, m0, 1024\n"
    "s_add_u32 s65, s65, 16384\n"
    "v_mfma_f32_16x16x32_bf16 v[92:95], a[112:115], a[216:219], v[92:95]\n"
    "s_waitcnt vmcnt(16)\n"
    "v_mfma_f32_16x16x32_bf16 a[224:227], a[120:123], a[160:163], a[224:227]\n"
    "buffer_load_dwordx4 a[112:115], v224, s[36:39], s60 offen offset:0 sc1\n"
    "v_mfma_f32_16x16x32_bf16 a[224:227], a[124:127], a[164:167], a[224:227]\n"
    "buffer_load_dwordx4 a[116:119], v224, s[36:39], s60 offen offset:1024 sc1\n"
    "v_mfma_f32_16x16x32_bf16 a[228:231], a[124:127], a[172:175], a[228:231]\n"
    "buffer_load_dwordx4 v231, s[40:43], s65 offen sc1 lds\n"
    "v_mfma_f32_16x16x32_bf16 a[228:231], a[120:123], a[168:171], a[228:231]\n"
    "v_mfma_f32_16x16x32_bf16 a[232:235], a[120:123], a[176:179], a[232:235]\n"
    "v_mfma_f32_16x16x32_bf16 a[232:235], a[124:127], a[180:183], a[232:235]\n"
    "v_mfma_f32_16x16x32_bf16 a[236:239], a[124:127], a[188:191], a[236:239]\n"
    "s_add_u32 m0, m0, 1024\n"
    "s_add_u32 s65, s65, 16384\n"
    "v_mfma_f32_16x16x32_bf16 a[236:239], a[120:123], a[184:187], a[236:239]\n"
    "v_mfma_f32_16x16x32_bf16 v[96:99], a[120:123], a[192:195], v[96:99]\n"
    "v_mfma_f32_16x16x32_bf16 v[96:99], a[124:127], a[196:199], v[96:99]\n"
    "v_mfma_f32_16x16x32_bf16 v[100:103], a[124:127], a[204:207], v[100:103]\n"
    "buffer_load_dwordx4 v231, s[40:43], s65 offen sc1 lds\n"
    "v_mfma_f32_16x16x32_bf16 v[100:103], a[120:123], a[200:203], v[100:103]\n"
    "v_mfma_f32_16x16x32_bf16 v[104:107], a[120:123], a[208:211], v[104:107]\n"
    "v_mfma_f32_16x16x32_bf16 v[104:107], a[124:127], a[212:215], v[104:107]\n"
    "v_mfma_f32_16x16x32_bf16 v[108:111], a[124:127], a[220:223], v[108:111]\n"
    "v_mfma_f32_16x16x32_bf16 v[108:111], a[120:123], a[216:219], v[108:111]\n"
    "s_add_u32 m0, m0, 1024\n"
    "s_waitcnt vmcnt(18)\n"
    "v_mfma_f32_16x16x32_bf16 v[112:115], a[128:131], a[160:163], v[112:115]\n"
    "buffer_load_dwordx4 a[120:123], v224, s[36:39], s60 offen offset:2048 sc1\n"
    "v_mfma_f32_16x16x32_bf16 v[112:115], a[132:135], a[164:167], v[112:115]\n"
    "buffer_load_dwordx4 a[124:127], v224, s[36:39], s60 offen offset:3072 sc1\n"
    "v_mfma_f32_16x16x32_bf16 v[116:119], a[132:135], a[172:175], v[116:119]\n"
    "s_add_u32 s60, s60, 4352\n"
    "v_mfma_f32_16x16x32_bf16 v[116:119], a[128:131], a[168:171], v[116:119]\n"
    "s_add_u32 s65, s65, 16384\n"
    "v_mfma_f32_16x16x32_bf16 v[120:123], a[128:131], a[176:179], v[120:123]\n"
    "v_mfma_f32_16x16x32_bf16 v[120:123], a[132:135], a[180:183], v[120:123]\n"
    "v_mfma_f32_16x16x32_bf16 v[124:127], a[132:135], a[188:191], v[124:127]\n"
    "v_mfma_f32_16x16x32_bf16 v[124:127], a[128:131], a[184:187], v[124:127]\n"
    "buffer_load_dwordx4 v231, s[40:43], s65 offen sc1 lds\n"
    "v_mfma_f32_16x16x32_bf16 v[128:131], a[128:131], a[192:195], v[128:131]\n"
    "v_mfma_f32_16x16x32_bf16 v[128:131], a[132:135], a[196:199], v[128:131]\n"
    "v_mfma_f32_16x16x32_bf16 v[132:135], a[132:135], a[204:207], v[132:135]\n"
    "v_mfma_f32_16x16x32_bf16 v[132:135], a[128:131], a[200:203], v[132:135]\n"
    "s_add_u32 m0, m0, 1024\n"
    "s_add_u32 s65, s65, 16384\n"
    "v_mfma_f32_16x16x32_bf16 v[136:139], a[128:131], a[208:211], v[136:139]\n"
    "v_mfma_f32_16x16x32_bf16 v[136:139], a[132:135], a[212:215], v[136:139]\n"
    "v_mfma_f32_16x16x32_bf16 v[140:143], a[132:135], a[220:223], v[140:143]\n"
    "s_add_u32 s63, s63, 32768\n"
    "s_and_b32 s63, s63, 0x1ffff\n"
    "v_mfma_f32_16x16x32_bf16 v[140:143], a[128:131], a[216:219], v[140:143]\n"
    "s_waitcnt vmcnt(19)\n"
    "v_mfma_f32_16x16x32_bf16 v[144:147], a[136:139], a[160:163], v[144:147]\n"
    "buffer_load_dwordx4 a[128:131], v224, s[36:39], s60 offen offset:0 sc1\n"
    "v_mfma_f32_16x16x32_bf16 v[144:147], a[140:143], a[164:167], v[144:147]\n"
    "buffer_load_dwordx4 a[132:135], v224, s[36:39], s60 offen offset:1024 sc1\n"
    "v_mfma_f32_16x16x32_bf16 v[148:151], a[140:143], a[172:175], v[148:151]\n"
    "v_mfma_f32_16x16x32_bf16 v[148:151], a[136:139], a[168:171], v[148:151]\n"
    "v_mfma_f32_16x16x32_bf16 v[152:155], a[136:139], a[176:179], v[152:155]\n"
    "buffer_load_dwordx4 v231, s[40:43], s65 offen sc1 lds\n"
    "v_mfma_f32_16x16x32_bf16 v[152:155], a[140:143], a[180:183], v[152:155]\n"
    "v_mfma_f32_16x16x32_bf16 v[156:159], a[140:143], a[188:191], v[156:159]\n"
    "v_mfma_f32_16x16x32_bf16 v[156:159], a[136:139], a[184:187], v[156:159]\n"
    "v_mfma_f32_16x16x32_bf16 v[160:163], a[136:139], a[192:195], v[160:163]\n"
    "s_add_u32 m0, m0, 1024\n"
    "s_add_u32 s65, s65, 16384\n"
    "v_mfma_f32_16x16x32_bf16 v[160:163], a[140:143], a[196:199], v[160:163]\n"
    "v_mfma_f32_16x16x32_bf16 v[164:167], a[140:143], a[204:207], v[164:167]\n"
    "v_mfma_f32_16x16x32_bf16 v[164:167], a[136:139], a[200:203], v[164:167]\n"
    "v_add_u32 v226, s63, v225\n"
    "v_xor_b32 v227, 64, v226\n"
    "v_mfma_f32_16x16x32_bf16 v[168:171], a[136:139], a[208:211], v[168:171]\n"
    "v_mfma_f32_16x16x32_bf16 v[168:171], a[140:143], a[212:215], v[168:171]\n"
    "buffer_load_dwordx4 v231, s[40:43], s65 offen sc1 lds\n"
    "v_mfma_f32_16x16x32_bf16 v[172:175], a[140:143], a[220:223], v[172:175]\n"
    "v_mfma_f32_16x16x32_bf16 v[172:175], a[136:139], a[216:219], v[172:175]\n"
    "s_waitcnt vmcnt(20)\n"
    "v_mfma_f32_16x16x32_bf16 v[176:179], a[144:147], a[160:163], v[176:179]\n"
    "buffer_load_dwordx4 a[136:139], v224, s[36:39], s60 offen offset:2048 sc1\n"
    "v_mfma_f32_16x16x32_bf16 v[176:179], a[148:151], a[164:167], v[176:179]\n"
    "buffer_load_dwordx4 a[140:143], v224, s[36:39], s60 offen offset:3072 sc1\n"
    "v_mfma_f32_16x16x32_bf16 v[180:183], a[148:151], a[172:175], v[180:183]\n"
    "s_add_u32 s60, s60, 4352\n"
    "v_mfma_f32_16x16x32_bf16 v[180:183], a[144:147], a[168:171], v[180:183]\n"
    "v_mfma_f32_16x16x32_bf16 v[184:187], a[144:147], a[176:179], v[184:187]\n"
    "v_mfma_f32_16x16x32_bf16 v[184:187], a[148:151], a[180:183], v[184:187]\n"
    "v_mfma_f32_16x16x32_bf16 v[188:191], a[148:151], a[188:191], v[188:191]\n"
    "v_mfma_f32_16x16x32_bf16 v[188:191], a[144:147], a[184:187], v[188:191]\n"
    "v_mfma_f32_16x16x32_bf16 v[192:195], a[144:147], a[192:195], v[192:195]\n"
    "v_mfma_f32_16x16x32_bf16 v[192:195], a[148:151], a[196:199], v[192:195]\n"
    "v_mfma_f32_16x16x32_bf16 v[196:199], a[148:151], a[204:207], v[196:199]\n"
    "v_mfma_f32_16x16x32_bf16 v[196:199], a[144:147], a[200:203], v[196:199]\n"
    "s_cmp_eq_u32 s62, 3\n"
    "s_cselect_b32 s81, s79, 0\n"
    "v_mfma_f32_16x16x32_bf16 v[200:203], a[144:147], a[208:211], v[200:203]\n"
    "v_mfma_f32_16x16x32_bf16 v[200:203], a[148:151], a[212:215], v[200:203]\n"
    "s_add_u32 s78, s78, s81\n"
    "s_sub_u32 s62, s62, 1\n"
    "v_mfma_f32_16x16x32_bf16 v[204:207], a[148:151], a[220:223], v[204:207]\n"
    "s_barrier\n"
    "v_mfma_f32_16x16x32_bf16 v[204:207], a[144:147], a[216:219], v[204:207]\n"
    "buffer_load_dwordx4 a[144:147], v224, s[36:39], s60 offen offset:0 sc1\n"
    "s_waitcnt vmcnt(21)\n"
    "v_mfma_f32_16x16x32_bf16 a[240:243], a[152:155], a[160:163], a[240:243]\n"
    "ds_read_b128 a[160:163], v226 offset:0\n"
    "buffer_load_dwordx4 a[148:151], v224, s[36:39], s60 offen offset:1024 sc1\n"
    "v_mfma_f32_16x16x32_bf16 a[240:243], a[156:159], a[164:167], a[240:243]\n"
    "ds_read_b128 a[164:167], v227 offset:0\n"
    "v_mfma_f32_16x16x32_bf16 a[244:247], a[156:159], a[172:175], a[244:247]\n"
    "ds_read_b128 a[172:175], v227 offset:2048\n"
    "v_mfma_f32_16x16x32_bf16 a[244:247], a[152:155], a[168:171], a[244:247]\n"
    "ds_read_b128 a[168:171], v226 offset:2048\n"
    "v_mfma_f32_16x16x32_bf16 a[248:251], a[152:155], a[176:179], a[248:251]\n"
    "ds_read_b128 a[176:179], v226 offset:4096\n"
    "v_mfma_f32_16x16x32_bf16 a[248:251], a[156:159], a[180:183], a[248:251]\n"
    "ds_read_b128 a[180:183], v227 offset:4096\n"
    "v_mfma_f32_16x16x32_bf16 a[252:255], a[156:159], a[188:191], a[252:255]\n"
    "ds_read_b128 a[188:191], v227 offset:6144\n"
    "v_mfma_f32_16x16x32_bf16 a[252:255], a[152:155], a[184:187], a[252:255]\n"
    "ds_read_b128 a[184:187], v226 offset:6144\n"
    "v_mfma_f32_16x16x32_bf16 v[208:211], a[152:155], a[192:195], v[208:211]\n"
    "ds_read_b128 a[192:195], v226 offset:8192\n"
    "v_mfma_f32_16x16x32_bf16 v[208:211], a[156:159], a[196:199], v[208:211]\n"
    "ds_read_b128 a[196:199], v227 offset:8192\n"
    "v_mfma_f32_16x16x32_bf16 v[212:215], a[156:159], a[204:207], v[212:215]\n"
    "ds_read_b128 a[204:207], v227 offset:10240\n"
    "v_mfma_f32_16x16x32_bf16 v[212:215], a[152:155], a[200:203], v[212:215]\n"
    "ds_read_b128 a[200:203], v226 offset:10240\n"
    "v_mfma_f32_16x16x32_bf16 v[216:219], a[152:155], a[208:211], v[216:219]\n"
    "ds_read_b128 a[208:211], v226 offset:12288\n"
    "v_mfma_f32_16x16x32_bf16 v[216:219], a[156:159], a[212:215], v[216:219]\n"
    "ds_read_b128 a[212:215], v227 offset:12288\n"
    "v_mfma_f32_16x16x32_bf16 v[220:223], a[156:159], a[220:223], v[220:223]\n"
    "ds_read_b128 a[220:223], v227 offset:14336\n"
    "v_mfma_f32_16x16x32_bf16 v[220:223], a[152:155], a[216:219], v[220:223]\n"
    "ds_read_b128 a[216:219], v226 offset:14336\n"
    "s_bitcmp1_b32 s16, s62\n"
    "s_cbranch_scc1 L_MB_%=\n"
    "s_cmp_eq_u32 s62, 5\n"
    "s_cbranch_scc1 L_HK5_%=\n"
    "s_cmp_eq_u32 s62, 4\n"
    "s_cbranch_scc1 L_HK4_%=\n"
    "s_cmp_eq_u32 s62, 3\n"
    "s_cbranch_scc1 L_HK3_%=\n"
    "s_cmp_eq_u32 s62, 2\n"
    "s_cbranch_scc1 L_HK2_%=\n"
    "s_cmp_eq_u32 s62, 1\n"
    "s_cbranch_scc1 L_HK1_%=\n"
    "s_waitcnt vmcnt(20) lgkmcnt(8)\n"
    "v_mfma_f32_16x16x32_bf16 v[0:3], a[96:99], a[160:163], v[0:3]\n"
    "buffer_load_dwordx4 a[152:155], v224, s[36:39], s60 offen offset:2048 sc1\n"
    "v_mfma_f32_16x16x32_bf16 v[0:3], a[100:103], a[164:167], v[0:3]\n"
    "buffer_load_dwordx4 a[156:159], v224, s[36:39], s60 offen offset:3072 sc1\n"
    "v_mfma_f32_16x16x32_bf16 v[4:7], a[100:103], a[172:175], v[4:7]\n"
    "s_add_u32 s60, s60, 4352\n"
    "v_mfma_f32_16x16x32_bf16 v[4:7], a[96:99], a[168:171], v[4:7]\n"
    "v_lshrrev_b32 v228, 3, v224\n"
    "v_and_b32 v228, 0x60, v228\n"
    "v_mfma_f32_16x16x32_bf16 v[8:11], a[96:99], a[176:179], v[8:11]\n"
    "v_mfma_f32_16x16x32_bf16 v[8:11], a[100:103], a[180:183], v[8:11]\n"
    "buffer_load_dwordx4 v[252:255], v228, s[56:59], s74 offen offset:128\n"
    "v_mfma_f32_16x16x32_bf16 v[12:15], a[100:103], a[188:191], v[12:15]\n"
    "buffer_load_dwordx4 v[236:239], v228, s[56:59], s74 offen offset:144\n"
    "v_mfma_f32_16x16x32_bf16 v[12:15], a[96:99], a[184:187], v[12:15]\n"
    "s_waitcnt lgkmcnt(0)\n"
    "v_mfma_f32_16x16x32_bf16 v[16:19], a[96:99], a[192:195], v[16:19]\n"
    "v_mfma_f32_16x16x32_bf16 v[16:19], a[100:103], a[196:199], v[16:19]\n"
    "v_mfma_f32_16x16x32_bf16 v[20:23], a[100:103], a[204:207], v[20:23]\n"
    "v_mfma_f32_16x16x32_bf16 v[20:23], a[96:99], a[200:203], v[20:23]\n"
    "v_mfma_f32_16x16x32_bf16 v[24:27], a[96:99], a[208:211], v[24:27]\n"
    "v_mfma_f32_16x16x32_bf16 v[24:27], a[100:103], a[212:215], v[24:27]\n"
    "v_mfma_f32_16x16x32_bf16 v[28:31], a[100:103], a[220:223], v[28:31]\n"
    "v_mfma_f32_16x16x32_bf16 v[28:31], a[96:99], a[216:219], v[28:31]\n"
    "s_waitcnt vmcnt(20)\n"
    "v_mfma_f32_16x16x32_bf16 v[32:35], a[104:107], a[160:163], v[32:35]\n"
    "buffer_load_dwordx4 a[96:99], v224, s[36:39], s60 offen offset:0 sc1\n"
    "v_mfma_f32_16x16x32_bf16 v[32:35], a[108:111], a[164:167], v[32:35]\n"
    "buffer_load_dwordx4 a[100:103], v224, s[36:39], s60 offen offset:1024 sc1\n"
    "v_mfma_f32_16x16x32_bf16 v[36:39], a[108:111], a[172:175], v[36:39]\n"
    "v_mfma_f32_16x16x32_bf16 v[36:39], a[104:107], a[168:171], v[36:39]\n"
    "v_mfma_f32_16x16x32_bf16 v[40:43], a[104:107], a[176:179], v[40:43]\n"
    "v_mfma_f32_16x16x32_bf16 v[40:43], a[108:111], a[180:183], v[40:43]\n"
    "v_mfma_f32_16x16x32_bf16 v[44:47], a[108:111], a[188:191], v[44:47]\n"
    "v_mfma_f32_16x16x32_bf16 v[44:47], a[104:107], a[184:187], v[44:47]\n"
    "v_mfma_f32_16x16x32_bf16 v[48:51], a[104:107], a[192:195], v[48:51]\n"
    "v_mfma_f32_16x16x32_bf16 v[48:51], a[108:111], a[196:199], v[48:51]\n"
    "v_mfma_f32_16x16x32_bf16 v[52:55], a[108:111], a[204:207], v[52:55]\n"
    "v_mfma_f32_16x16x32_bf16 v[52:55], a[104:107], a[200:203], v[52:55]\n"
    "v_mfma_f32_16x16x32_bf16 v[56:59], a[104:107], a[208:211], v[56:59]\n"
    "v_mfma_f32_16x16x32_bf16 v[56:59], a[108:111], a[212:215], v[56:59]\n"
    "v_mfma_f32_16x16x32_bf16 v[60:63], a[108:111], a[220:223], v[60:63]\n"
    "v_mfma_f32_16x16x32_bf16 v[60:63], a[104:107], a[216:219], v[60:63]\n"
    "s_waitcnt vmcnt(19)\n"
    "v_mfma_f32_16x16x32_bf16 v[64:67], a[112:115], a[160:163], v[64:67]\n"
    "buffer_load_dwordx4 a[104:107], v224, s[36:39], s60 offen offset:2048 sc1\n"
    "v_mfma_f32_16x16x32_bf16 v[64:67], a[116:119], a[164:167], v[64:67]\n"
    "buffer_load_dwordx4 a[108:111], v224, s[36:39], s60 offen offset:3072 sc1\n"
    "v_mfma_f32_16x16x32_bf16 v[68:71], a[116:119], a[172:175], v[68:71]\n"
    "s_add_u32 s60, s60, 4352\n"
    "v_mfma_f32_16x16x32_bf16 v[68:71], a[112:115], a[168:171], v[68:71]\n"
    "v_mfma_f32_16x16x32_bf16 v[72:75], a[112:115], a[176:179], v[72:75]\n"
    "v_mfma_f32_16x16x32_bf16 v[72:75], a[116:119], a[180:183], v[72:75]\n"
    "v_mfma_f32_16x16x32_bf16 v[76:79], a[116:119], a[188:191], v[76:79]\n"
    "v_mfma_f32_16x16x32_bf16 v[76:79], a[112:115], a[184:187], v[76:79]\n"
    "v_mfma_f32_16x16x32_bf16 v[80:83], a[112:115], a[192:195], v[80:83]\n"
    "v_mfma_f32_16x16x32_bf16 v[80:83], a[116:119], a[196:199], v[80:83]\n"
    "v_mfma_f32_16x16x32_bf16 v[84:87], a[116:119], a[204:207], v[84:87]\n"
    "v_mfma_f32_16x16x32_bf16 v[84:87], a[112:115], a[200:203], v[84:87]\n"
    "v_mfma_f32_16x16x32_bf16 v[88:91], a[112:115], a[208:211], v[88:91]\n"
    "v_mfma_f32_16x16x32_bf16 v[88:91], a[116:119], a[212:215], v[88:91]\n"
    "v_mfma_f32_16x16x32_bf16 v[92:95], a[116:119], a[220:223], v[92:95]\n"
    "v_mfma_f32_16x16x32_bf16 v[92:95], a[112:115], a[216:219], v[92:95]\n"
    "buffer_load_dwordx4 a[112:115], v224, s[36:39], s60 offen offset:0 sc1\n"
    "s_waitcnt vmcnt(18)\n"
    "v_mfma_f32_16x16x32_bf16 a[224:227], a[120:123], a[160:163], a[224:227]\n"
    "ds_read_b128 a[160:163], v226 offset:16384\n"
    "buffer_load_dwordx4 a[116:119], v224, s[36:39], s60 offen offset:1024 sc1\n"
    "v_mfma_f32_16x16x32_bf16 a[224:227], a[124:127], a[164:167], a[224:227]\n"
    "ds_read_b128 a[164:167], v227 offset:16384\n"
    "v_mfma_f32_16x16x32_bf16 a[228:231], a[124:127], a[172:175], a[228:231]\n"
    "ds_read_b128 a[172:175], v227 offset:18432\n"
    "v_mfma_f32_16x16x32_bf16 a[228:231], a[120:123], a[168:171], a[228:231]\n"
    "ds_read_b128 a[168:171], v226 offset:18432\n"
    "v_mfma_f32_16x16x32_bf16 a[232:235], a[120:123], a[176:179], a[232:235]\n"
    "ds_read_b128 a[176:179], v226 offset:20480\n"
    "v_mfma_f32_16x16x32_bf16 a[232:235], a[124:127], a[180:183], a[232:235]\n"
    "ds_read_b128 a[180:183], v227 offset:20480\n"
    "v_mfma_f32_16x16x32_bf16 a[236:239], a[124:127], a[188:191], a[236:239]\n"
    "ds_read_b128 a[188:191], v227 offset:22528\n"
    "v_mfma_f32_16x16x32_bf16 a[236:239], a[120:123], a[184:187], a[236:239]\n"
    "ds_read_b128 a[184:187], v226 offset:22528\n"
    "v_mfma_f32_16x16x32_bf16 v[96:99], a[120:123], a[192:195], v[96:99]\n"
    "ds_read_b128 a[192:195], v226 offset:24576\n"
    "v_mfma_f32_16x16x32_bf16 v[96:99], a[124:127], a[196:199], v[96:99]\n"
    "ds_read_b128 a[196:199], v227 offset:24576\n"
    "v_mfma_f32_16x16x32_bf16 v[100:103], a[124:127], a[204:207], v[100:103]\n"
    "ds_read_b128 a[204:207], v227 offset:26624\n"
    "v_mfma_f32_16x16x32_bf16 v[100:103], a[120:123], a[200:203], v[100:103]\n"
    "ds_read_b128 a[200:203], v226 offset:26624\n"
    "v_mfma_f32_16x16x32_bf16 v[104:107], a[120:123], a[208:211], v[104:107]\n"
    "ds_read_b128 a[208:211], v226 offset:28672\n"
    "v_mfma_f32_16x16x32_bf16 v[104:107], a[124:127], a[212:215], v[104:107]\n"
    "ds_read_b128 a[212:215], v227 offset:28672\n"
    "v_mfma_f32_16x16x32_bf16 v[108:111], a[124:127], a[220:223], v[108:111]\n"
    "ds_read_b128 a[220:223], v227 offset:30720\n"
    "v_mfma_f32_16x16x32_bf16 v[108:111], a[120:123], a[216:219], v[108:111]\n"
    "ds_read_b128 a[216:219], v226 offset:30720\n"
    "s_waitcnt vmcnt(16) lgkmcnt(8)\n"
    "v_mfma_f32_16x16x32_bf16 v[0:3], a[128:131], a[160:163], v[0:3]\n"
    "buffer_load_dwordx4 a[120:123], v224, s[36:39], s60 offen offset:2048 sc1\n"
    "v_mfma_f32_16x16x32_bf16 v[0:3], a[132:135], a[164:167], v[0:3]\n"
    "buffer_load_dwordx4 a[124:127], v224, s[36:39], s60 offen offset:3072 sc1\n"
    "v_mfma_f32_16x16x32_bf16 v[4:7], a[132:135], a[172:175], v[4:7]\n"
    "s_add_u32 s60, s60, 4352\n"
    "v_mfma_f32_16x16x32_bf16 v[4:7], a[128:131], a[168:171], v[4:7]\n"
    "v_mfma_f32_16x16x32_bf16 v[8:11], a[128:131], a[176:179], v[8:11]\n"
    "v_mfma_f32_16x16x32_bf16 v[8:11], a[132:135], a[180:183], v[8:11]\n"
    "v_mfma_f32_16x16x32_bf16 v[12:15], a[132:135], a[188:191], v[12:15]\n"
    "v_mfma_f32_16x16x32_bf16 v[12:15], a[128:131], a[184:187], v[12:15]\n"
    "s_waitcnt vmcnt(9)\n"
    "v_lshlrev_b32 v244, 16, v252\n"
    "s_waitcnt lgkmcnt(0)\n"
    "v_mfma_f32_16x16x32_bf16 v[16:19], a[128:131], a[192:195], v[16:19]\n"
    "v_and_b32 v245, s73, v252\n"
    "v_lshlrev_b32 v246, 16, v253\n"
    "v_mfma_f32_16x16x32_bf16 v[16:19], a[132:135], a[196:199], v[16:19]\n"
    "v_mfma_f32_16x16x32_bf16 v[20:23], a[132:135], a[204:207], v[20:23]\n"
    "v_and_b32 v247, s73, v253\n"
    "v_lshlrev_b32 v248, 16, v254\n"
    "v_mfma_f32_16x16x32_bf16 v[20:23], a[128:131], a[200:203], v[20:23]\n"
    "v_and_b32 v249, s73, v254\n"
    "v_lshlrev_b32 v250, 16, v255\n"
    "v_mfma_f32_16x16x32_bf16 v[24:27], a[128:131], a[208:211], v[24:27]\n"
    "v_mfma_f32_16x16x32_bf16 v[24:27], a[132:135], a[212:215], v[24:27]\n"
    "v_and_b32 v251, s73, v255\n"
    "v_add_f32 v112, v244, v112\n"
    "v_mfma_f32_16x16x32_bf16 v[28:31], a[132:135], a[220:223], v[28:31]\n"
    "v_add_f32 v144, v245, v144\n"
    "v_add_f32 v176, v246, v176\n"
    "v_mfma_f32_16x16x32_bf16 v[28:31], a[128:131], a[216:219], v[28:31]\n"
    "v_mfma_f32_16x16x32_bf16 v[32:35], a[136:139], a[160:163], v[32:35]\n"
    "buffer_load_dwordx4 a[128:131], v224, s[36:39], s60 offen offset:0 sc1\n"
    "v_mfma_f32_16x16x32_bf16 v[32:35], a[140:143], a[164:167], v[32:35]\n"
    "buffer_load_dwordx4 a[132:135], v224, s[36:39], s60 offen offset:1024 sc1\n"
    "v_mfma_f32_16x16x32_bf16 v[36:39], a[140:143], a[172:175], v[36:39]\n"
    "v_mfma_f32_16x16x32_bf16 v[36:39], a[136:139], a[168:171], v[36:39]\n"
    "v_accvgpr_read_b32 v229, a240\n"
    "v_add_f32 v229, v247, v229\n"
    "v_mfma_f32_16x16x32_bf16 v[40:43], a[136:139], a[176:179], v[40:43]\n"
    "v_accvgpr_write_b32 a240, v229\n"
    "v_add_f32 v113, v248, v113\n"
    "v_mfma_f32_16x16x32_bf16 v[40:43], a[140:143], a[180:183], v[40:43]\n"
    "v_add_f32 v145, v249, v145\n"
    "v_add_f32 v177, v250, v177\n"
    "v_mfma_f32_16x16x32_bf16 v[44:47], a[140:143], a[188:191], v[44:47]\n"
    "v_mfma_f32_16x16x32_bf16 v[44:47], a[136:139], a[184:187], v[44:47]\n"
    "v_accvgpr_read_b32 v229, a241\n"
    "v_add_f32 v229, v251, v229\n"
    "v_mfma_f32_16x16x32_bf16 v[48:51], a[136:139], a[192:195], v[48:51]\n"
    "v_accvgpr_write_b32 a241, v229\n"
    "v_add_f32 v116, v244, v116\n"
    "v_mfma_f32_16x16x32_bf16 v[48:51], a[140:143], a[196:199], v[48:51]\n"
    "v_mfma_f32_16x16x32_bf16 v[52:55], a[140:143], a[204:207], v[52:55]\n"
    "v_add_f32 v148, v245, v148\n"
    "v_add_f32 v180, v246, v180\n"
    "v_mfma_f32_16x16x32_bf16 v[52:55], a[136:139], a[200:203], v[52:55]\n"
    "v_accvgpr_read_b32 v229, a244\n"
    "v_add_f32 v229, v247, v229\n"
    "v_mfma_f32_16x16x32_bf16 v[56:59], a[136:139], a[208:211], v[56:59]\n"
    "v_mfma_f32_16x16x32_bf16 v[56:59], a[140:143], a[212:215], v[56:59]\n"
    "v_accvgpr_write_b32 a244, v229\n"
    "v_add_f32 v117, v248, v117\n"
    "v_mfma_f32_16x16x32_bf16 v[60:63], a[140:143], a[220:223], v[60:63]\n"
    "v_add_f32 v149, v249, v149\n"
    "v_add_f32 v181, v250, v181\n"
    "v_mfma_f32_16x16x32_bf16 v[60:63], a[136:139], a[216:219], v[60:63]\n"
    "v_mfma_f32_16x16x32_bf16 v[64:67], a[144:147], a[160:163], v[64:67]\n"
    "buffer_load_dwordx4 a[136:139], v224, s[36:39], s60 offen offset:2048 sc1\n"
    "v_mfma_f32_16x16x32_bf16 v[64:67], a[148:151], a[164:167], v[64:67]\n"
    "buffer_load_dwordx4 a[140:143], v224, s[36:39], s60 offen offset:3072 sc1\n"
    "v_mfma_f32_16x16x32_bf16 v[68:71], a[148:151], a[172:175], v[68:71]\n"
    "s_add_u32 s60, s60, 4352\n"
    "v_mfma_f32_16x16x32_bf16 v[68:71], a[144:147], a[168:171], v[68:71]\n"
    "v_accvgpr_read_b32 v229, a245\n"
    "v_add_f32 v229, v251, v229\n"
    "v_mfma_f32_16x16x32_bf16 v[72:75], a[144:147], a[176:179], v[72:75]\n"
    "v_accvgpr_write_b32 a245, v229\n"
    "v_add_f32 v120, v244, v120\n"
    "v_mfma_f32_16x16x32_bf16 v[72:75], a[148:151], a[180:183], v[72:75]\n"
    "v_add_f32 v152, v245, v152\n"
    "v_add_f32 v184, v246, v184\n"
    "v_mfma_f32_16x16x32_bf16 v[76:79], a[148:151], a[188:191], v[76:79]\n"
    "v_mfma_f32_16x16x32_bf16 v[76:79], a[144:147], a[184:187], v[76:79]\n"
    "v_accvgpr_read_b32 v229, a248\n"
    "v_add_f32 v229, v247, v229\n"
    "v_mfma_f32_16x16x32_bf16 v[80:83], a[144:147], a[192:195], v[80:83]\n"
    "v_accvgpr_write_b32 a248, v229\n"
    "v_add_f32 v121, v248, v121\n"
    "v_mfma_f32_16x16x32_bf16 v[80:83], a[148:151], a[196:199], v[80:83]\n"
    "v_mfma_f32_16x16x32_bf16 v[84:87], a[148:151], a[204:207], v[84:87]\n"
    "v_add_f32 v153, v249, v153\n"
    "v_add_f32 v185, v250, v185\n"
    "v_mfma_f32_16x16x32_bf16 v[84:87], a[144:147], a[200:203], v[84:87]\n"
    "v_accvgpr_read_b32 v229, a249\n"
    "v_add_f32 v229, v251, v229\n"
    "v_mfma_f32_16x16x32_bf16 v[88:91], a[144:147], a[208:211], v[88:91]\n"
    "v_mfma_f32_16x16x32_bf16 v[88:91], a[148:151], a[212:215], v[88:91]\n"
    "v_accvgpr_write_b32 a249, v229\n"
    "v_add_f32 v124, v244, v124\n"
    "v_mfma_f32_16x16x32_bf16 v[92:95], a[148:151], a[220:223], v[92:95]\n"
    "s_barrier\n"
    "v_mfma_f32_16x16x32_bf16 v[92:95], a[144:147], a[216:219], v[92:95]\n"
    "buffer_load_dwordx4 a[144:147], v224, s[36:39], s60 offen offset:0 sc1\n"
    "v_mfma_f32_16x16x32_bf16 a[224:227], a[152:155], a[160:163], a[224:227]\n"
    "ds_read_b128 a[160:163], v226 offset:32768\n"
    "buffer_load_dwordx4 a[148:151], v224, s[36:39], s60 offen offset:1024 sc1\n"
    "v_mfma_f32_16x16x32_bf16 a[224:227], a[156:159], a[164:167], a[224:227]\n"
    "ds_read_b128 a[164:167], v227 offset:32768\n"
    "v_add_f32 v156, v245, v156\n"
    "v_mfma_f32_16x16x32_bf16 a[228:231], a[156:159], a[172:175], a[228:231]\n"
    "ds_read_b128 a[172:175], v227 offset:34816\n"
    "v_mfma_f32_16x16x32_bf16 a[228:231], a[152:155], a[168:171], a[228:231]\n"
    "ds_read_b128 a[168:171], v226 offset:34816\n"
    "v_add_f32 v188, v246, v188\n"
    "v_mfma_f32_16x16x32_bf16 a[232:235], a[152:155], a[176:179], a[232:235]\n"
    "ds_read_b128 a[176:179], v226 offset:36864\n"
    "v_accvgpr_read_b32 v229, a252\n"
    "v_mfma_f32_16x16x32_bf16 a[232:235], a[156:159], a[180:183], a[232:235]\n"
    "ds_read_b128 a[180:183], v227 offset:36864\n"
    "v_add_f32 v229, v247, v229\n"
    "v_mfma_f32_16x16x32_bf16 a[236:239], a[156:159], a[188:191], a[236:239]\n"
    "ds_read_b128 a[188:191], v227 offset:38912\n"
    "v_mfma_f32_16x16x32_bf16 a[236:239], a[152:155], a[184:187], a[236:239]\n"
    "ds_read_b128 a[184:187], v226 offset:38912\n"
    "v_accvgpr_write_b32 a252, v229\n"
    "v_mfma_f32_16x16x32_bf16 v[96:99], a[152:155], a[192:195], v[96:99]\n"
    "ds_read_b128 a[192:195], v226 offset:40960\n"
    "v_add_f32 v125, v248, v125\n"
    "v_mfma_f32_16x16x32_bf16 v[96:99], a[156:159], a[196:199], v[96:99]\n"
    "ds_read_b128 a[196:199], v227 offset:40960\n"
    "v_mfma_f32_16x16x32_bf16 v[100:103], a[156:159], a[204:207], v[100:103]\n"
    "ds_read_b128 a[204:207], v227 offset:43008\n"
    "v_add_f32 v157, v249, v157\n"
    "v_mfma_f32_16x16x32_bf16 v[100:103], a[152:155], a[200:203], v[100:103]\n"
    "ds_read_b128 a[200:203], v226 offset:43008\n"
    "v_add_f32 v189, v250, v189\n"
    "v_mfma_f32_16x16x32_bf16 v[104:107], a[152:155], a[208:211], v[104:107]\n"
    "ds_read_b128 a[208:211], v226 offset:45056\n"
    "v_mfma_f32_16x16x32_bf16 v[104:107], a[156:159], a[212:215], v[104:107]\n"
    "ds_read_b128 a[212:215], v227 offset:45056\n"
    "v_accvgpr_read_b32 v229, a253\n"
    "v_mfma_f32_16x16x32_bf16 v[108:111], a[156:159], a[220:223], v[108:111]\n"
    "ds_read_b128 a[220:223], v227 offset:47104\n"
    "v_add_f32 v229, v251, v229\n"
    "v_mfma_f32_16x16x32_bf16 v[108:111], a[152:155], a[216:219], v[108:111]\n"
    "ds_read_b128 a[216:219], v226 offset:47104\n"
    "s_waitcnt vmcnt(12) lgkmcnt(8)\n"
    "v_mfma_f32_16x16x32_bf16 v[0:3], a[96:99], a[160:163], v[0:3]\n"
    "buffer_load_dwordx4 a[152:155], v224, s[36:39], s60 offen offset:2048 sc1\n"
    "v_mfma_f32_16x16x32_bf16 v[0:3], a[100:103], a[164:167], v[0:3]\n"
    "buffer_load_dwordx4 a[156:159], v224, s[36:39], s60 offen offset:3072 sc1\n"
    "v_mfma_f32_16x16x32_bf16 v[4:7], a[100:103], a[172:175], v[4:7]\n"
    "s_add_u32 s60, s60, 4352\n"
    "v_mfma_f32_16x16x32_bf16 v[4:7], a[96:99], a[168:171], v[4:7]\n"
    "v_accvgpr_write_b32 a253, v229\n"
    "v_add_f32 v128, v244, v128\n"
    "v_mfma_f32_16x16x32_bf16 v[8:11], a[96:99], a[176:179], v[8:11]\n"
    "v_add_f32 v160, v245, v160\n"
    "v_add_f32 v192, v246, v192\n"
    "v_mfma_f32_16x16x32_bf16 v[8:11], a[100:103], a[180:183], v[8:11]\n"
    "v_add_f32 v208, v247, v208\n"
    "v_add_f32 v129, v248, v129\n"
    "v_mfma_f32_16x16x32_bf16 v[12:15], a[100:103], a[188:191], v[12:15]\n"
    "v_mfma_f32_16x16x32_bf16 v[12:15], a[96:99], a[184:187], v[12:15]\n"
    "v_add_f32 v161, v249, v161\n"
    "s_waitcnt lgkmcnt(0)\n"
    "v_mfma_f32_16x16x32_bf16 v[16:19], a[96:99], a[192:195], v[16:19]\n"
    "v_add_f32 v193, v250, v193\n"
    "v_add_f32 v209, v251, v209\n"
    "v_mfma_f32_16x16x32_bf16 v[16:19], a[100:103], a[196:199], v[16:19]\n"
    "v_mfma_f32_16x16x32_bf16 v[20:23], a[100:103], a[204:207], v[20:23]\n"
    "v_add_f32 v132, v244, v132\n"
    "v_add_f32 v164, v245, v164\n"
    "v_mfma_f32_16x16x32_bf16 v[20:23], a[96:99], a[200:203], v[20:23]\n"
    "v_add_f32 v196, v246, v196\n"
    "v_add_f32 v212, v247, v212\n"
    "v_mfma_f32_16x16x32_bf16 v[24:27], a[96:99], a[208:211], v[24:27]\n"
    "v_mfma_f32_16x16x32_bf16 v[24:27], a[100:103], a[212:215], v[24:27]\n"
    "v_add_f32 v133, v248, v133\n"
    "v_add_f32 v165, v249, v165\n"
    "v_mfma_f32_16x16x32_bf16 v[28:31], a[100:103], a[220:223], v[28:31]\n"
    "v_add_f32 v197, v250, v197\n"
    "v_add_f32 v213, v251, v213\n"
    "v_mfma_f32_16x16x32_bf16 v[28:31], a[96:99], a[216:219], v[28:31]\n"
    "s_waitcnt vmcnt(12)\n"
    "v_mfma_f32_16x16x32_bf16 v[32:35], a[104:107], a[160:163], v[32:35]\n"
    "buffer_load_dwordx4 a[96:99], v224, s[36:39], s60 offen offset:0 sc1\n"
    "v_mfma_f32_16x16x32_bf16 v[32:35], a[108:111], a[164:167], v[32:35]\n"
    "buffer_load_dwordx4 a[100:103], v224, s[36:39], s60 offen offset:1024 sc1\n"
    "v_mfma_f32_16x16x32_bf16 v[36:39], a[108:111], a[172:175], v[36:39]\n"
    "v_mfma_f32_16x16x32_bf16 v[36:39], a[104:107], a[168:171], v[36:39]\n"
    "v_add_f32 v136, v244, v136\n"
    "v_add_f32 v168, v245, v168\n"
    "v_mfma_f32_16x16x32_bf16 v[40:43], a[104:107], a[176:179], v[40:43]\n"
    "v_add_f32 v200, v246, v200\n"
    "v_add_f32 v216, v247, v216\n"
    "v_mfma_f32_16x16x32_bf16 v[40:43], a[108:111], a[180:183], v[40:43]\n"
    "v_add_f32 v137, v248, v137\n"
    "v_add_f32 v169, v249, v169\n"
    "v_mfma_f32_16x16x32_bf16 v[44:47], a[108:111], a[188:191], v[44:47]\n"
    "v_mfma_f32_16x16x32_bf16 v[44:47], a[104:107], a[184:187], v[44:47]\n"
    "v_add_f32 v201, v250, v201\n"
    "v_add_f32 v217, v251, v217\n"
    "v_mfma_f32_16x16x32_bf16 v[48:51], a[104:107], a[192:195], v[48:51]\n"
    "v_add_f32 v140, v244, v140\n"
    "v_add_f32 v172, v245, v172\n"
    "v_mfma_f32_16x16x32_bf16 v[48:51], a[108:111], a[196:199], v[48:51]\n"
    "v_mfma_f32_16x16x32_bf16 v[52:55], a[108:111], a[204:207], v[52:55]\n"
    "v_add_f32 v204, v246, v204\n"
    "v_add_f32 v220, v247, v220\n"
    "v_mfma_f32_16x16x32_bf16 v[52:55], a[104:107], a[200:203], v[52:55]\n"
    "v_add_f32 v141, v248, v141\n"
    "v_add_f32 v173, v249, v173\n"
    "v_mfma_f32_16x16x32_bf16 v[56:59], a[104:107], a[208:211], v[56:59]\n"
    "v_mfma_f32_16x16x32_bf16 v[56:59], a[108:111], a[212:215], v[56:59]\n"
    "v_add_f32 v205, v250, v205\n"
    "v_add_f32 v221, v251, v221\n"
    "v_mfma_f32_16x16x32_bf16 v[60:63], a[108:111], a[220:223], v[60:63]\n"
    "v_lshlrev_b32 v244, 16, v236\n"
    "v_and_b32 v245, s73, v236\n"
    "v_mfma_f32_16x16x32_bf16 v[60:63], a[104:107], a[216:219], v[60:63]\n"
    "s_waitcnt vmcnt(12)\n"
    "v_mfma_f32_16x16x32_bf16 v[64:67], a[112:115], a[160:163], v[64:67]\n"
    "buffer_load_dwordx4 a[104:107], v224, s[36:39], s60 offen offset:2048 sc1\n"
    "v_mfma_f32_16x16x32_bf16 v[64:67], a[116:119], a[164:167], v[64:67]\n"
    "buffer_load_dwordx4 a[108:111], v224, s[36:39], s60 offen offset:3072 sc1\n"
    "v_mfma_f32_16x16x32_bf16 v[68:71], a[116:119], a[172:175], v[68:71]\n"
    "s_add_u32 s60, s60, 4352\n"
    "v_mfma_f32_16x16x32_bf16 v[68:71], a[112:115], a[168:171], v[68:71]\n"
    "v_lshlrev_b32 v246, 16, v237\n"
    "v_and_b32 v247, s73, v237\n"
    "v_mfma_f32_16x16x32_bf16 v[72:75], a[112:115], a[176:179], v[72:75]\n"
    "v_lshlrev_b32 v248, 16, v238\n"
    "v_and_b32 v249, s73, v238\n"
    "v_mfma_f32_16x16x32_bf16 v[72:75], a[116:119], a[180:183], v[72:75]\n"
    "v_lshlrev_b32 v250, 16, v239\n"
    "v_and_b32 v251, s73, v239\n"
    "v_mfma_f32_16x16x32_bf16 v[76:79], a[116:119], a[188:191], v[76:79]\n"
    "v_mfma_f32_16x16x32_bf16 v[76:79], a[112:115], a[184:187], v[76:79]\n"
    "v_add_f32 v114, v244, v114\n"
    "v_add_f32 v146, v245, v146\n"
    "v_mfma_f32_16x16x32_bf16 v[80:83], a[112:115], a[192:195], v[80:83]\n"
    "v_add_f32 v178, v246, v178\n"
    "v_accvgpr_read_b32 v229, a242\n"
    "v_mfma_f32_16x16x32_bf16 v[80:83], a[116:119], a[196:199], v[80:83]\n"
    "v_mfma_f32_16x16x32_bf16 v[84:87], a[116:119], a[204:207], v[84:87]\n"
    "v_add_f32 v229, v247, v229\n"
    "v_accvgpr_write_b32 a242, v229\n"
    "v_mfma_f32_16x16x32_bf16 v[84:87], a[112:115], a[200:203], v[84:87]\n"
    "v_add_f32 v115, v248, v115\n"
    "v_add_f32 v147, v249, v147\n"
    "v_mfma_f32_16x16x32_bf16 v[88:91], a[112:115], a[208:211], v[88:91]\n"
    "v_mfma_f32_16x16x32_bf16 v[88:91], a[116:119], a[212:215], v[88:91]\n"
    "v_add_f32 v179, v250, v179\n"
    "v_accvgpr_read_b32 v229, a243\n"
    "v_mfma_f32_16x16x32_bf16 v[92:95], a[116:119], a[220:223], v[92:95]\n"
    "v_add_f32 v229, v251, v229\n"
    "v_accvgpr_write_b32 a243, v229\n"
    "v_mfma_f32_16x16x32_bf16 v[92:95], a[112:115], a[216:219], v[92:95]\n"
    "buffer_load_dwordx4 a[112:115], v224, s[36:39], s60 offen offset:0 sc1\n"
    "s_waitcnt vmcnt(13)\n"
    "v_mfma_f32_16x16x32_bf16 a[224:227], a[120:123], a[160:163], a[224:227]\n"
    "ds_read_b128 a[160:163], v226 offset:49152\n"
    "buffer_load_dwordx4 a[116:119], v224, s[36:39], s60 offen offset:1024 sc1\n"
    "v_mfma_f32_16x16x32_bf16 a[224:227], a[124:127], a[164:167], a[224:227]\n"
    "ds_read_b128 a[164:167], v227 offset:49152\n"
    "v_add_f32 v118, v244, v118\n"
    "v_mfma_f32_16x16x32_bf16 a[228:231], a[124:127], a[172:175], a[228:231]\n"
    "ds_read_b128 a[172:175], v227 offset:51200\n"
    "v_mfma_f32_16x16x32_bf16 a[228:231], a[120:123], a[168:171], a[228:231]\n"
    "ds_read_b128 a[168:171], v226 offset:51200\n"
    "v_add_f32 v150, v245, v150\n"
    "v_mfma_f32_16x16x32_bf16 a[232:235], a[120:123], a[176:179], a[232:235]\n"
    "ds_read_b128 a[176:179], v226 offset:53248\n"
    "v_add_f32 v182, v246, v182\n"
    "v_mfma_f32_16x16x32_bf16 a[232:235], a[124:127], a[180:183], a[232:235]\n"
    "ds_read_b128 a[180:183], v227 offset:53248\n"
    "v_accvgpr_read_b32 v229, a246\n"
    "v_mfma_f32_16x16x32_bf16 a[236:239], a[124:127], a[188:191], a[236:239]\n"
    "ds_read_b128 a[188:191], v227 offset:55296\n"
    "v_mfma_f32_16x16x32_bf16 a[236:239], a[120:123], a[184:187], a[236:239]\n"
    "ds_read_b128 a[184:187], v226 offset:55296\n"
    "v_add_f32 v229, v247, v229\n"
    "v_mfma_f32_16x16x32_bf16 v[96:99], a[120:123], a[192:195], v[96:99]\n"
    "ds_read_b128 a[192:195], v226 offset:57344\n"
    "v_accvgpr_write_b32 a246, v229\n"
    "v_mfma_f32_16x16x32_bf16 v[96:99], a[124:127], a[196:199], v[96:99]\n"
    "ds_read_b128 a[196:199], v227 offset:57344\n"
    "v_mfma_f32_16x16x32_bf16 v[100:103], a[124:127], a[204:207], v[100:103]\n"
    "ds_read_b128 a[204:207], v227 offset:59392\n"
    "v_add_f32 v119, v248, v119\n"
    "v_mfma_f32_16x16x32_bf16 v[100:103], a[120:123], a[200:203], v[100:103]\n"
    "ds_read_b128 a[200:203], v226 offset:59392\n"
    "v_add_f32 v151, v249, v151\n"
    "v_mfma_f32_16x16x32_bf16 v[104:107], a[120:123], a[208:211], v[104:107]\n"
    "ds_read_b128 a[208:211], v226 offset:61440\n"
    "v_mfma_f32_16x16x32_bf16 v[104:107], a[124:127], a[212:215], v[104:107]\n"
    "ds_read_b128 a[212:215], v227 offset:61440\n"
    "v_add_f32 v183, v250, v183\n"
    "v_mfma_f32_16x16x32_bf16 v[108:111], a[124:127], a[220:223], v[108:111]\n"
    "ds_read_b128 a[220:223], v227 offset:63488\n"
    "v_accvgpr_read_b32 v229, a247\n"
    "v_mfma_f32_16x16x32_bf16 v[108:111], a[120:123], a[216:219], v[108:111]\n"
    "ds_read_b128 a[216:219], v226 offset:63488\n"
    "s_waitcnt vmcnt(12) lgkmcnt(8)\n"
    "v_mfma_f32_16x16x32_bf16 v[0:3], a[128:131], a[160:163], v[0:3]\n"
    "buffer_load_dwordx4 a[120:123], v224, s[36:39], s60 offen offset:2048 sc1\n"
    "v_mfma_f32_16x16x32_bf16 v[0:3], a[132:135], a[164:167], v[0:3]\n"
    "buffer_load_dwordx4 a[124:127], v224, s[36:39], s60 offen offset:3072 sc1\n"
    "v_mfma_f32_16x16x32_bf16 v[4:7], a[132:135], a[172:175], v[4:7]\n"
    "s_add_u32 s60, s60, 4352\n"
    "v_mfma_f32_16x16x32_bf16 v[4:7], a[128:131], a[168:171], v[4:7]\n"
    "v_add_f32 v229, v251, v229\n"
    "v_accvgpr_write_b32 a247, v229\n"
    "v_mfma_f32_16x16x32_bf16 v[8:11], a[128:131], a[176:179], v[8:11]\n"
    "v_add_f32 v122, v244, v122\n"
    "v_add_f32 v154, v245, v154\n"
    "v_mfma_f32_16x16x32_bf16 v[8:11], a[132:135], a[180:183], v[8:11]\n"
    "v_add_f32 v186, v246, v186\n"
    "v_accvgpr_read_b32 v229, a250\n"
    "v_mfma_f32_16x16x32_bf16 v[12:15], a[132:135], a[188:191], v[12:15]\n"
    "v_mfma_f32_16x16x32_bf16 v[12:15], a[128:131], a[184:187], v[12:15]\n"
    "v_add_f32 v229, v247, v229\n"
    "s_waitcnt lgkmcnt(0)\n"
    "v_mfma_f32_16x16x32_bf16 v[16:19], a[128:131], a[192:195], v[16:19]\n"
    "v_accvgpr_write_b32 a250, v229\n"
    "v_add_f32 v123, v248, v123\n"
    "v_mfma_f32_16x16x32_bf16 v[16:19], a[132:135], a[196:199], v[16:19]\n"
    "v_mfma_f32_16x16x32_bf16 v[20:23], a[132:135], a[204:207], v[20:23]\n"
    "v_add_f32 v155, v249, v155\n"
    "v_add_f32 v187, v250, v187\n"
    "v_mfma_f32_16x16x32_bf16 v[20:23], a[128:131], a[200:203], v[20:23]\n"
    "v_accvgpr_read_b32 v229, a251\n"
    "v_add_f32 v229, v251, v229\n"
    "v_mfma_f32_16x16x32_bf16 v[24:27], a[128:131], a[208:211], v[24:27]\n"
    "v_mfma_f32_16x16x32_bf16 v[24:27], a[132:135], a[212:215], v[24:27]\n"
    "v_accvgpr_write_b32 a251, v229\n"
    "v_add_f32 v126, v244, v126\n"
    "v_mfma_f32_16x16x32_bf16 v[28:31], a[132:135], a[220:223], v[28:31]\n"
    "v_add_f32 v158, v245, v158\n"
    "v_add_f32 v190, v246, v190\n"
    "v_mfma_f32_16x16x32_bf16 v[28:31], a[128:131], a[216:219], v[28:31]\n"
    "s_waitcnt vmcnt(12)\n"
    "v_mfma_f32_16x16x32_bf16 v[32:35], a[136:139], a[160:163], v[32:35]\n"
    "buffer_load_dwordx4 a[128:131], v224, s[36:39], s60 offen offset:0 sc1\n"
    "v_mfma_f32_16x16x32_bf16 v[32:35], a[140:143], a[164:167], v[32:35]\n"
    "buffer_load_dwordx4 a[132:135], v224, s[36:39], s60 offen offset:1024 sc1\n"
    "v_mfma_f32_16x16x32_bf16 v[36:39], a[140:143], a[172:175], v[36:39]\n"
    "v_mfma_f32_16x16x32_bf16 v[36:39], a[136:139], a[168:171], v[36:39]\n"
    "v_accvgpr_read_b32 v229, a254\n"
    "v_add_f32 v229, v247, v229\n"
    "v_mfma_f32_16x16x32_bf16 v[40:43], a[136:139], a[176:179], v[40:43]\n"
    "v_accvgpr_write_b32 a254, v229\n"
    "v_add_f32 v127, v248, v127\n"
    "v_mfma_f32_16x16x32_bf16 v[40:43], a[140:143], a[180:183], v[40:43]\n"
    "v_add_f32 v159, v249, v159\n"
    "v_add_f32 v191, v250, v191\n"
    "v_mfma_f32_16x16x32_bf16 v[44:47], a[140:143], a[188:191], v[44:47]\n"
    "v_mfma_f32_16x16x32_bf16 v[44:47], a[136:139], a[184:187], v[44:47]\n"
    "v_accvgpr_read_b32 v229, a255\n"
    "v_add_f32 v229, v251, v229\n"
    "v_mfma_f32_16x16x32_bf16 v[48:51], a[136:139], a[192:195], v[48:51]\n"
    "v_accvgpr_write_b32 a255, v229\n"
    "v_add_f32 v130, v244, v130\n"
    "v_mfma_f32_16x16x32_bf16 v[48:51], a[140:143], a[196:199], v[48:51]\n"
    "v_mfma_f32_16x16x32_bf16 v[52:55], a[140:143], a[204:207], v[52:55]\n"
    "v_add_f32 v162, v245, v162\n"
    "v_add_f32 v194, v246, v194\n"
    "v_mfma_f32_16x16x32_bf16 v[52:55], a[136:139], a[200:203], v[52:55]\n"
    "v_add_f32 v210, v247, v210\n"
    "v_add_f32 v131, v248, v131\n"
    "v_mfma_f32_16x16x32_bf16 v[56:59], a[136:139], a[208:211], v[56:59]\n"
    "v_mfma_f32_16x16x32_bf16 v[56:59], a[140:143], a[212:215], v[56:59]\n"
    "v_add_f32 v163, v249, v163\n"
    "v_add_f32 v195, v250, v195\n"
    "v_mfma_f32_16x16x32_bf16 v[60:63], a[140:143], a[220:223], v[60:63]\n"
    "v_add_f32 v211, v251, v211\n"
    "v_add_f32 v134, v244, v134\n"
    "v_mfma_f32_16x16x32_bf16 v[60:63], a[136:139], a[216:219], v[60:63]\n"
    "s_waitcnt vmcnt(12)\n"
    "v_mfma_f32_16x16x32_bf16 v[64:67], a[144:147], a[160:163], v[64:67]\n"
    "buffer_load_dwordx4 a[136:139], v224, s[36:39], s60 offen offset:2048 sc1\n"
    "v_mfma_f32_16x16x32_bf16 v[64:67], a[148:151], a[164:167], v[64:67]\n"
    "buffer_load_dwordx4 a[140:143], v224, s[36:39], s60 offen offset:3072 sc1\n"
    "v_mfma_f32_16x16x32_bf16 v[68:71], a[148:151], a[172:175], v[68:71]\n"
    "s_add_u32 s60, s60, 4352\n"
    "v_mfma_f32_16x16x32_bf16 v[68:71], a[144:147], a[168:171], v[68:71]\n"
    "v_add_f32 v166, v245, v166\n"
    "v_add_f32 v198, v246, v198\n"
    "v_mfma_f32_16x16x32_bf16 v[72:75], a[144:147], a[176:179], v[72:75]\n"
    "v_add_f32 v214, v247, v214\n"
    "v_add_f32 v135, v248, v135\n"
    "v_mfma_f32_16x16x32_bf16 v[72:75], a[148:151], a[180:183], v[72:75]\n"
    "v_add_f32 v167, v249, v167\n"
    "v_add_f32 v199, v250, v199\n"
    "v_mfma_f32_16x16x32_bf16 v[76:79], a[148:151], a[188:191], v[76:79]\n"
    "v_mfma_f32_16x16x32_bf16 v[76:79], a[144:147], a[184:187], v[76:79]\n"
    "v_add_f32 v215, v251, v215\n"
    "v_add_f32 v138, v244, v138\n"
    "v_mfma_f32_16x16x32_bf16 v[80:83], a[144:147], a[192:195], v[80:83]\n"
    "v_add_f32 v170, v245, v170\n"
    "v_add_f32 v202, v246, v202\n"
    "v_mfma_f32_16x16x32_bf16 v[80:83], a[148:151], a[196:199], v[80:83]\n"
    "v_mfma_f32_16x16x32_bf16 v[84:87], a[148:151], a[204:207], v[84:87]\n"
    "v_add_f32 v218, v247, v218\n"
    "v_add_f32 v139, v248, v139\n"
    "v_mfma_f32_16x16x32_bf16 v[84:87], a[144:147], a[200:203], v[84:87]\n"
    "v_add_f32 v171, v249, v171\n"
    "v_add_f32 v203, v250, v203\n"
    "v_mfma_f32_16x16x32_bf16 v[88:91], a[144:147], a[208:211], v[88:91]\n"
    "v_mfma_f32_16x16x32_bf16 v[88:91], a[148:151], a[212:215], v[88:91]\n"
    "v_add_f32 v219, v251, v219\n"
    "v_add_f32 v142, v244, v142\n"
    "v_mfma_f32_16x16x32_bf16 v[92:95], a[148:151], a[220:223], v[92:95]\n"
    "v_add_f32 v174, v245, v174\n"
    "v_add_f32 v206, v246, v206\n"
    "v_mfma_f32_16x16x32_bf16 v[92:95], a[144:147], a[216:219], v[92:95]\n"
    "buffer_load_dwordx4 a[144:147], v224, s[36:39], s60 offen offset:0 sc1\n"
    "s_waitcnt vmcnt(13)\n"
    "v_mfma_f32_16x16x32_bf16 a[224:227], a[152:155], a[160:163], a[224:227]\n"
    "ds_read_b128 a[160:163], v226 offset:0\n"
    "buffer_load_dwordx4 a[148:151], v224, s[36:39], s60 offen offset:1024 sc1\n"
    "v_mfma_f32_16x16x32_bf16 a[224:227], a[156:159], a[164:167], a[224:227]\n"
    "ds_read_b128 a[164:167], v227 offset:0\n"
    "v_add_f32 v222, v247, v222\n"
    "v_mfma_f32_16x16x32_bf16 a[228:231], a[156:159], a[172:175], a[228:231]\n"
    "ds_read_b128 a[172:175], v227 offset:2048\n"
    "v_mfma_f32_16x16x32_bf16 a[228:231], a[152:155], a[168:171], a[228:231]\n"
    "ds_read_b128 a[168:171], v226 offset:2048\n"
    "v_add_f32 v143, v248, v143\n"
    "v_mfma_f32_16x16x32_bf16 a[232:235], a[152:155], a[176:179], a[232:235]\n"
    "ds_read_b128 a[176:179], v226 offset:4096\n"
    "v_add_f32 v175, v249, v175\n"
    "v_mfma_f32_16x16x32_bf16 a[232:235], a[156:159], a[180:183], a[232:235]\n"
    "ds_read_b128 a[180:183], v227 offset:4096\n"
    "v_add_f32 v207, v250, v207\n"
    "v_mfma_f32_16x16x32_bf16 a[236:239], a[156:159], a[188:191], a[236:239]\n"
    "ds_read_b128 a[188:191], v227 offset:6144\n"
    "v_mfma_f32_16x16x32_bf16 a[236:239], a[152:155], a[184:187], a[236:239]\n"
    "ds_read_b128 a[184:187], v226 offset:6144\n"
    "v_add_f32 v223, v251, v223\n"
    "v_mfma_f32_16x16x32_bf16 v[96:99], a[152:155], a[192:195], v[96:99]\n"
    "ds_read_b128 a[192:195], v226 offset:8192\n"
    "v_mfma_f32_16x16x32_bf16 v[96:99], a[156:159], a[196:199], v[96:99]\n"
    "ds_read_b128 a[196:199], v227 offset:8192\n"
    "v_mfma_f32_16x16x32_bf16 v[100:103], a[156:159], a[204:207], v[100:103]\n"
    "ds_read_b128 a[204:207], v227 offset:10240\n"
    "v_mfma_f32_16x16x32_bf16 v[100:103], a[152:155], a[200:203], v[100:103]\n"
    "ds_read_b128 a[200:203], v226 offset:10240\n"
    "v_mfma_f32_16x16x32_bf16 v[104:107], a[152:155], a[208:211], v[104:107]\n"
    "ds_read_b128 a[208:211], v226 offset:12288\n"
    "v_mfma_f32_16x16x32_bf16 v[104:107], a[156:159], a[212:215], v[104:107]\n"
    "ds_read_b128 a[212:215], v227 offset:12288\n"
    "v_mfma_f32_16x16x32_bf16 v[108:111], a[156:159], a[220:223], v[108:111]\n"
    "ds_read_b128 a[220:223], v227 offset:14336\n"
    "v_mfma_f32_16x16x32_bf16 v[108:111], a[152:155], a[216:219], v[108:111]\n"
    "ds_read_b128 a[216:219], v226 offset:14336\n"
    "s_cmp_lg_u32 s61, 0\n"
    "s_cbranch_scc1 L_NOCAPB_%=\n"
    "v_add_u32 v228, s72, v230\n"
    "v_xor_b32 v229, 16, v228\n"
    "ds_read_b128 a[48:51], v228 offset:0\n"
    "ds_read_b128 a[52:55], v228 offset:2048\n"
    "ds_read_b128 a[56:59], v228 offset:4096\n"
    "ds_read_b128 a[60:63], v228 offset:6144\n"
    "ds_read_b128 a[64:67], v228 offset:8192\n"
    "ds_read_b128 a[68:71], v228 offset:10240\n"
    "ds_read_b128 a[72:75], v228 offset:12288\n"
    "ds_read_b128 a[76:79], v228 offset:14336\n"
    "ds_read_b128 a[80:83], v229 offset:0\n"
    "ds_read_b128 a[84:87], v229 offset:2048\n"
    "ds_read_b128 a[88:91], v229 offset:4096\n"
    "ds_read_b128 a[92:95], v229 offset:6144\n"
    "ds_read_b128 v[236:239], v229 offset:8192\n"
    "ds_read_b128 v[240:243], v229 offset:10240\n"
    "ds_read_b128 v[244:247], v229 offset:12288\n"
    "ds_read_b128 v[248:251], v229 offset:14336\n"
    "s_add_u32 s17, s77, 16384\n"
    "s_add_u32 s18, s77, 32768\n"
    "s_add_u32 s19, s77, 49152\n"
    "s_add_u32 s20, s77, 65536\n"
    "s_add_u32 s21, s77, 81920\n"
    "s_add_u32 s22, s77, 98304\n"
    "s_add_u32 s23, s77, 114688\n"
    "s_add_u32 s24, s77, 131072\n"
    "s_add_u32 s25, s77, 147456\n"
    "s_add_u32 s26, s77, 163840\n"
    "s_add_u32 s27, s77, 180224\n"
    "s_add_u32 s28, s77, 196608\n"
    "s_add_u32 s29, s77, 212992\n"
    "s_add_u32 s30, s77, 229376\n"
    "s_add_u32 s31, s77, 245760\n"
    "s_waitcnt lgkmcnt(3)\n"
    "ds_write_b128 v235, v[236:239] offset:4096\n"
    "s_waitcnt lgkmcnt(3)\n"
    "ds_write_b128 v235, v[240:243] offset:5120\n"
    "s_waitcnt lgkmcnt(3)\n"
    "ds_write_b128 v235, v[244:247] offset:6144\n"
    "s_waitcnt lgkmcnt(3)\n"
    "ds_write_b128 v235, v[248:251] offset:7168\n"
    "s_branch L_E1B_%=\n"
    "L_NOCAPB_%=:\n"
    "s_mov_b32 s44, s86\n"
    "s_mov_b32 s45, s87\n"
    "s_mov_b32 s86, s88\n"
    "s_mov_b32 s87, s89\n"
    "L_E1B_%=:\n"
    "s_waitcnt vmcnt(12) lgkmcnt(8)\n"
    "v_mfma_f32_16x16x32_bf16 v[112:115], a[96:99], a[160:163], v[112:115]\n"
    "buffer_load_dwordx4 a[152:155], v224, s[36:39], s60 offen offset:2048 sc1\n"
    "v_mfma_f32_16x16x32_bf16 v[112:115], a[100:103], a[164:167], v[112:115]\n"
    "buffer_load_dwordx4 a[156:159], v224, s[36:39], s60 offen offset:3072 sc1\n"
    "v_mfma_f32_16x16x32_bf16 v[116:119], a[100:103], a[172:175], v[116:119]\n"
    "s_add_u32 s60, s60, 4352\n"
    "v_add_u32 v228, s70, v230\n"
    "v_mfma_f32_16x16x32_bf16 v[116:119], a[96:99], a[168:171], v[116:119]\n"
    "v_xor_b32 v229, 16, v228\n"
    "v_add_u32 v232, s70, v224\n"
    "v_mfma_f32_16x16x32_bf16 v[120:123], a[96:99], a[176:179], v[120:123]\n"
    "v_accvgpr_read_b32 v244, a0\n"
    "v_accvgpr_read_b32 v246, a1\n"
    "v_mfma_f32_16x16x32_bf16 v[120:123], a[100:103], a[180:183], v[120:123]\n"
    "v_accvgpr_read_b32 v237, a224\n"
    "v_lshlrev_b32 v245, 16, v244\n"
    "v_lshlrev_b32 v247, 16, v246\n"
    "v_mfma_f32_16x16x32_bf16 v[124:127], a[100:103], a[188:191], v[124:127]\n"
    "v_and_b32 v244, s73, v244\n"
    "v_and_b32 v246, s73, v246\n"
    "v_mfma_f32_16x16x32_bf16 v[124:127], a[96:99], a[184:187], v[124:127]\n"
    "v_mul_f32 v245, v245, v0\n"
    "v_mul_f32 v247, v247, v64\n"
    "s_waitcnt lgkmcnt(0)\n"
    "v_mfma_f32_16x16x32_bf16 v[128:131], a[96:99], a[192:195], v[128:131]\n"
    "v_mul_f32 v244, v244, v32\n"
    "v_mul_f32 v246, v246, v237\n"
    "v_mfma_f32_16x16x32_bf16 v[128:131], a[100:103], a[196:199], v[128:131]\n"
    "v_cvt_pk_bf16_f32 v236, v245, v244\n"
    "v_cvt_pk_bf16_f32 v237, v247, v246\n"
    "v_accvgpr_read_b32 v244, a2\n"
    "v_mfma_f32_16x16x32_bf16 v[132:135], a[100:103], a[204:207], v[132:135]\n"
    "v_accvgpr_read_b32 v246, a3\n"
    "v_accvgpr_read_b32 v239, a225\n"
    "v_mfma_f32_16x16x32_bf16 v[132:135], a[96:99], a[200:203], v[132:135]\n"
    "v_lshlrev_b32 v245, 16, v244\n"
    "v_lshlrev_b32 v247, 16, v246\n"
    "v_and_b32 v244, s73, v244\n"
    "v_mfma_f32_16x16x32_bf16 v[136:139], a[96:99], a[208:211], v[136:139]\n"
    "v_and_b32 v246, s73, v246\n"
    "v_mul_f32 v245, v245, v1\n"
    "v_mfma_f32_16x16x32_bf16 v[136:139], a[100:103], a[212:215], v[136:139]\n"
    "v_mul_f32 v247, v247, v65\n"
    "v_mul_f32 v244, v244, v33\n"
    "v_mul_f32 v246, v246, v239\n"
    "v_mfma_f32_16x16x32_bf16 v[140:143], a[100:103], a[220:223], v[140:143]\n"
    "v_cvt_pk_bf16_f32 v238, v245, v244\n"
    "v_cvt_pk_bf16_f32 v239, v247, v246\n"
    "v_mfma_f32_16x16x32_bf16 v[140:143], a[96:99], a[216:219], v[140:143]\n"
    "ds_write_b128 v228, v[236:239] offset:0\n"
    "s_waitcnt vmcnt(12)\n"
    "v_mfma_f32_16x16x32_bf16 v[144:147], a[104:107], a[160:163], v[144:147]\n"
    "buffer_load_dwordx4 a[96:99], v224, s[36:39], s60 offen offset:0 sc1\n"
    "v_mfma_f32_16x16x32_bf16 v[144:147], a[108:111], a[164:167], v[144:147]\n"
    "buffer_load_dwordx4 a[100:103], v224, s[36:39], s60 offen offset:1024 sc1\n"
    "v_accvgpr_read_b32 v244, a32\n"
    "v_mfma_f32_16x16x32_bf16 v[148:151], a[108:111], a[172:175], v[148:151]\n"
    "v_accvgpr_read_b32 v246, a33\n"
    "v_accvgpr_read_b32 v237, a226\n"
    "v_mfma_f32_16x16x32_bf16 v[148:151], a[104:107], a[168:171], v[148:151]\n"
    "v_lshlrev_b32 v245, 16, v244\n"
    "v_lshlrev_b32 v247, 16, v246\n"
    "v_and_b32 v244, s73, v244\n"
    "v_mfma_f32_16x16x32_bf16 v[152:155], a[104:107], a[176:179], v[152:155]\n"
    "v_and_b32 v246, s73, v246\n"
    "v_mul_f32 v245, v245, v2\n"
    "v_mfma_f32_16x16x32_bf16 v[152:155], a[108:111], a[180:183], v[152:155]\n"
    "v_mul_f32 v247, v247, v66\n"
    "v_mul_f32 v244, v244, v34\n"
    "v_mul_f32 v246, v246, v237\n"
    "v_mfma_f32_16x16x32_bf16 v[156:159], a[108:111], a[188:191], v[156:159]\n"
    "v_cvt_pk_bf16_f32 v236, v245, v244\n"
    "v_cvt_pk_bf16_f32 v237, v247, v246\n"
    "v_mfma_f32_16x16x32_bf16 v[156:159], a[104:107], a[184:187], v[156:159]\n"
    "v_accvgpr_read_b32 v244, a34\n"
    "v_accvgpr_read_b32 v246, a35\n"
    "v_accvgpr_read_b32 v239, a227\n"
    "v_mfma_f32_16x16x32_bf16 v[160:163], a[104:107], a[192:195], v[160:163]\n"
    "v_lshlrev_b32 v245, 16, v244\n"
    "v_lshlrev_b32 v247, 16, v246\n"
    "v_mfma_f32_16x16x32_bf16 v[160:163], a[108:111], a[196:199], v[160:163]\n"
    "v_and_b32 v244, s73, v244\n"
    "v_and_b32 v246, s73, v246\n"
    "v_mul_f32 v245, v245, v3\n"
    "v_mfma_f32_16x16x32_bf16 v[164:167], a[108:111], a[204:207], v[164:167]\n"
    "v_mul_f32 v247, v247, v67\n"
    "v_mul_f32 v244, v244, v35\n"
    "v_mfma_f32_16x16x32_bf16 v[164:167], a[104:107], a[200:203], v[164:167]\n"
    "v_mul_f32 v246, v246, v239\n"
    "v_cvt_pk_bf16_f32 v238, v245, v244\n"
    "v_cvt_pk_bf16_f32 v239, v247, v246\n"
    "v_mfma_f32_16x16x32_bf16 v[168:171], a[104:107], a[208:211], v[168:171]\n"
    "ds_write_b128 v229, v[236:239] offset:0\n"
    "v_mfma_f32_16x16x32_bf16 v[168:171], a[108:111], a[212:215], v[168:171]\n"
    "ds_read_b128 v[240:243], v232 offset:0\n"
    "ds_read_b128 v[252:255], v232 offset:1024\n"
    "v_accvgpr_read_b32 v244, a4\n"
    "v_mfma_f32_16x16x32_bf16 v[172:175], a[108:111], a[220:223], v[172:175]\n"
    "v_accvgpr_read_b32 v246, a5\n"
    "v_accvgpr_read_b32 v237, a228\n"
    "v_mfma_f32_16x16x32_bf16 v[172:175], a[104:107], a[216:219], v[172:175]\n"
    "v_lshlrev_b32 v245, 16, v244\n"
    "v_lshlrev_b32 v247, 16, v246\n"
    "s_waitcnt vmcnt(12)\n"
    "v_mfma_f32_16x16x32_bf16 v[176:179], a[112:115], a[160:163], v[176:179]\n"
    "buffer_load_dwordx4 a[104:107], v224, s[36:39], s60 offen offset:2048 sc1\n"
    "v_mfma_f32_16x16x32_bf16 v[176:179], a[116:119], a[164:167], v[176:179]\n"
    "buffer_load_dwordx4 a[108:111], v224, s[36:39], s60 offen offset:3072 sc1\n"
    "v_and_b32 v244, s73, v244\n"
    "v_mfma_f32_16x16x32_bf16 v[180:183], a[116:119], a[172:175], v[180:183]\n"
    "s_add_u32 s60, s60, 4352\n"
    "v_and_b32 v246, s73, v246\n"
    "v_mfma_f32_16x16x32_bf16 v[180:183], a[112:115], a[168:171], v[180:183]\n"
    "v_mul_f32 v245, v245, v4\n"
    "v_mul_f32 v247, v247, v68\n"
    "v_mul_f32 v244, v244, v36\n"
    "v_mfma_f32_16x16x32_bf16 v[184:187], a[112:115], a[176:179], v[184:187]\n"
    "v_mul_f32 v246, v246, v237\n"
    "v_cvt_pk_bf16_f32 v236, v245, v244\n"
    "v_cvt_pk_bf16_f32 v237, v247, v246\n"
    "v_mfma_f32_16x16x32_bf16 v[184:187], a[116:119], a[180:183], v[184:187]\n"
    "v_accvgpr_read_b32 v244, a6\n"
    "v_accvgpr_read_b32 v246, a7\n"
    "v_mfma_f32_16x16x32_bf16 v[188:191], a[116:119], a[188:191], v[188:191]\n"
    "v_accvgpr_read_b32 v239, a229\n"
    "v_lshlrev_b32 v245, 16, v244\n"
    "v_lshlrev_b32 v247, 16, v246\n"
    "v_mfma_f32_16x16x32_bf16 v[188:191], a[112:115], a[184:187], v[188:191]\n"
    "v_and_b32 v244, s73, v244\n"
    "v_and_b32 v246, s73, v246\n"
    "v_mfma_f32_16x16x32_bf16 v[192:195], a[112:115], a[192:195], v[192:195]\n"
    "v_mul_f32 v245, v245, v5\n"
    "v_mul_f32 v247, v247, v69\n"
    "v_mul_f32 v244, v244, v37\n"
    "v_mfma_f32_16x16x32_bf16 v[192:195], a[116:119], a[196:199], v[192:195]\n"
    "s_waitcnt lgkmcnt(1)\n"
    "buffer_store_dwordx4 v[240:243], v231, s[44:47], s77 offen offset:0 sc1\n"
    "v_mfma_f32_16x16x32_bf16 v[196:199], a[116:119], a[204:207], v[196:199]\n"
    "v_mul_f32 v246, v246, v239\n"
    "v_cvt_pk_bf16_f32 v238, v245, v244\n"
    "v_cvt_pk_bf16_f32 v239, v247, v246\n"
    "v_mfma_f32_16x16x32_bf16 v[196:199], a[112:115], a[200:203], v[196:199]\n"
    "ds_write_b128 v228, v[236:239] offset:2048\n"
    "v_mfma_f32_16x16x32_bf16 v[200:203], a[112:115], a[208:211], v[200:203]\n"
    "v_accvgpr_read_b32 v244, a36\n"
    "v_accvgpr_read_b32 v246, a37\n"
    "v_accvgpr_read_b32 v237, a230\n"
    "v_mfma_f32_16x16x32_bf16 v[200:203], a[116:119], a[212:215], v[200:203]\n"
    "v_lshlrev_b32 v245, 16, v244\n"
    "v_lshlrev_b32 v247, 16, v246\n"
    "v_mfma_f32_16x16x32_bf16 v[204:207], a[116:119], a[220:223], v[204:207]\n"
    "v_and_b32 v244, s73, v244\n"
    "v_and_b32 v246, s73, v246\n"
    "v_mul_f32 v245, v245, v6\n"
    "v_mfma_f32_16x16x32_bf16 v[204:207], a[112:115], a[216:219], v[204:207]\n"
    "buffer_load_dwordx4 a[112:115], v224, s[36:39], s60 offen offset:0 sc1\n"
    "s_waitcnt vmcnt(14)\n"
    "v_mfma_f32_16x16x32_bf16 a[240:243], a[120:123], a[160:163], a[240:243]\n"
    "ds_read_b128 a[160:163], v226 offset:16384\n"
    "buffer_load_dwordx4 a[116:119], v224, s[36:39], s60 offen offset:1024 sc1\n"
    "v_mfma_f32_16x16x32_bf16 a[240:243], a[124:127], a[164:167], a[240:243]\n"
    "ds_read_b128 a[164:167], v227 offset:16384\n"
    "v_mul_f32 v247, v247, v70\n"
    "v_mfma_f32_16x16x32_bf16 a[244:247], a[124:127], a[172:175], a[244:247]\n"
    "ds_read_b128 a[172:175], v227 offset:18432\n"
    "v_mul_f32 v244, v244, v38\n"
    "v_mul_f32 v246, v246, v237\n"
    "v_mfma_f32_16x16x32_bf16 a[244:247], a[120:123], a[168:171], a[244:247]\n"
    "ds_read_b128 a[168:171], v226 offset:18432\n"
    "v_cvt_pk_bf16_f32 v236, v245, v244\n"
    "v_mfma_f32_16x16x32_bf16 a[248:251], a[120:123], a[176:179], a[248:251]\n"
    "ds_read_b128 a[176:179], v226 offset:20480\n"
    "v_cvt_pk_bf16_f32 v237, v247, v246\n"
    "v_accvgpr_read_b32 v244, a38\n"
    "v_mfma_f32_16x16x32_bf16 a[248:251], a[124:127], a[180:183], a[248:251]\n"
    "ds_read_b128 a[180:183], v227 offset:20480\n"
    "s_waitcnt lgkmcnt(7)\n"
    "buffer_store_dwordx4 v[252:255], v231, s[44:47], s17 offen offset:0 sc1\n"
    "v_mfma_f32_16x16x32_bf16 a[252:255], a[124:127], a[188:191], a[252:255]\n"
    "ds_read_b128 a[188:191], v227 offset:22528\n"
    "v_accvgpr_read_b32 v246, a39\n"
    "v_accvgpr_read_b32 v239, a231\n"
    "v_mfma_f32_16x16x32_bf16 a[252:255], a[120:123], a[184:187], a[252:255]\n"
    "ds_read_b128 a[184:187], v226 offset:22528\n"
    "v_lshlrev_b32 v245, 16, v244\n"
    "v_mfma_f32_16x16x32_bf16 v[208:211], a[120:123], a[192:195], v[208:211]\n"
    "ds_read_b128 a[192:195], v226 offset:24576\n"
    "v_lshlrev_b32 v247, 16, v246\n"
    "v_and_b32 v244, s73, v244\n"
    "v_mfma_f32_16x16x32_bf16 v[208:211], a[124:127], a[196:199], v[208:211]\n"
    "ds_read_b128 a[196:199], v227 offset:24576\n"
    "v_and_b32 v246, s73, v246\n"
    "v_mfma_f32_16x16x32_bf16 v[212:215], a[124:127], a[204:207], v[212:215]\n"
    "ds_read_b128 a[204:207], v227 offset:26624\n"
    "v_mul_f32 v245, v245, v7\n"
    "v_mul_f32 v247, v247, v71\n"
    "v_mfma_f32_16x16x32_bf16 v[212:215], a[120:123], a[200:203], v[212:215]\n"
    "ds_read_b128 a[200:203], v226 offset:26624\n"
    "v_mul_f32 v244, v244, v39\n"
    "v_mfma_f32_16x16x32_bf16 v[216:219], a[120:123], a[208:211], v[216:219]\n"
    "ds_read_b128 a[208:211], v226 offset:28672\n"
    "v_mul_f32 v246, v246, v239\n"
    "v_cvt_pk_bf16_f32 v238, v245, v244\n"
    "v_mfma_f32_16x16x32_bf16 v[216:219], a[124:127], a[212:215], v[216:219]\n"
    "ds_read_b128 a[212:215], v227 offset:28672\n"
    "v_cvt_pk_bf16_f32 v239, v247, v246\n"
    "v_mfma_f32_16x16x32_bf16 v[220:223], a[124:127], a[220:223], v[220:223]\n"
    "ds_read_b128 a[220:223], v227 offset:30720\n"
    "ds_write_b128 v229, v[236:239] offset:2048\n"
    "v_mfma_f32_16x16x32_bf16 v[220:223], a[120:123], a[216:219], v[220:223]\n"
    "ds_read_b128 a[216:219], v226 offset:30720\n"
    "s_waitcnt vmcnt(14) lgkmcnt(9)\n"
    "v_mfma_f32_16x16x32_bf16 v[112:115], a[128:131], a[160:163], v[112:115]\n"
    "buffer_load_dwordx4 a[120:123], v224, s[36:39], s60 offen offset:2048 sc1\n"
    "ds_read_b128 v[240:243], v232 offset:2048\n"
    "v_mfma_f32_16x16x32_bf16 v[112:115], a[132:135], a[164:167], v[112:115]\n"
    "buffer_load_dwordx4 a[124:127], v224, s[36:39], s60 offen offset:3072 sc1\n"
    "v_mfma_f32_16x16x32_bf16 v[116:119], a[132:135], a[172:175], v[116:119]\n"
    "s_add_u32 s60, s60, 4352\n"
    "ds_read_b128 v[252:255], v232 offset:3072\n"
    "v_accvgpr_read_b32 v244, a8\n"
    "v_mfma_f32_16x16x32_bf16 v[116:119], a[128:131], a[168:171], v[116:119]\n"
    "v_accvgpr_read_b32 v246, a9\n"
    "v_accvgpr_read_b32 v237, a232\n"
    "v_lshlrev_b32 v245, 16, v244\n"
    "v_mfma_f32_16x16x32_bf16 v[120:123], a[128:131], a[176:179], v[120:123]\n"
    "v_lshlrev_b32 v247, 16, v246\n"
    "v_and_b32 v244, s73, v244\n"
    "v_mfma_f32_16x16x32_bf16 v[120:123], a[132:135], a[180:183], v[120:123]\n"
    "v_and_b32 v246, s73, v246\n"
    "v_mul_f32 v245, v245, v8\n"
    "v_mul_f32 v247, v247, v72\n"
    "v_mfma_f32_16x16x32_bf16 v[124:127], a[132:135], a[188:191], v[124:127]\n"
    "v_mul_f32 v244, v244, v40\n"
    "v_mul_f32 v246, v246, v237\n"
    "v_mfma_f32_16x16x32_bf16 v[124:127], a[128:131], a[184:187], v[124:127]\n"
    "v_cvt_pk_bf16_f32 v236, v245, v244\n"
    "v_cvt_pk_bf16_f32 v237, v247, v246\n"
    "s_waitcnt lgkmcnt(2)\n"
    "v_mfma_f32_16x16x32_bf16 v[128:131], a[128:131], a[192:195], v[128:131]\n"
    "v_accvgpr_read_b32 v244, a10\n"
    "v_accvgpr_read_b32 v246, a11\n"
    "v_mfma_f32_16x16x32_bf16 v[128:131], a[132:135], a[196:199], v[128:131]\n"
    "v_accvgpr_read_b32 v239, a233\n"
    "v_lshlrev_b32 v245, 16, v244\n"
    "v_lshlrev_b32 v247, 16, v246\n"
    "v_mfma_f32_16x16x32_bf16 v[132:135], a[132:135], a[204:207], v[132:135]\n"
    "v_and_b32 v244, s73, v244\n"
    "v_and_b32 v246, s73, v246\n"
    "v_mfma_f32_16x16x32_bf16 v[132:135], a[128:131], a[200:203], v[132:135]\n"
    "v_mul_f32 v245, v245, v9\n"
    "v_mul_f32 v247, v247, v73\n"
    "v_mul_f32 v244, v244, v41\n"
    "v_mfma_f32_16x16x32_bf16 v[136:139], a[128:131], a[208:211], v[136:139]\n"
    "s_waitcnt lgkmcnt(1)\n"
    "buffer_store_dwordx4 v[240:243], v231, s[44:47], s18 offen offset:0 sc1\n"
    "v_mfma_f32_16x16x32_bf16 v[136:139], a[132:135], a[212:215], v[136:139]\n"
    "v_mul_f32 v246, v246, v239\n"
    "v_cvt_pk_bf16_f32 v238, v245, v244\n"
    "v_cvt_pk_bf16_f32 v239, v247, v246\n"
    "v_mfma_f32_16x16x32_bf16 v[140:143], a[132:135], a[220:223], v[140:143]\n"
    "ds_write_b128 v228, v[236:239] offset:4096\n"
    "v_mfma_f32_16x16x32_bf16 v[140:143], a[128:131], a[216:219], v[140:143]\n"
    "v_accvgpr_read_b32 v244, a40\n"
    "v_accvgpr_read_b32 v246, a41\n"
    "s_waitcnt vmcnt(15)\n"
    "v_mfma_f32_16x16x32_bf16 v[144:147], a[136:139], a[160:163], v[144:147]\n"
    "buffer_load_dwordx4 a[128:131], v224, s[36:39], s60 offen offset:0 sc1\n"
    "v_mfma_f32_16x16x32_bf16 v[144:147], a[140:143], a[164:167], v[144:147]\n"
    "buffer_load_dwordx4 a[132:135], v224, s[36:39], s60 offen offset:1024 sc1\n"
    "v_accvgpr_read_b32 v237, a234\n"
    "v_mfma_f32_16x16x32_bf16 v[148:151], a[140:143], a[172:175], v[148:151]\n"
    "v_lshlrev_b32 v245, 16, v244\n"
    "v_lshlrev_b32 v247, 16, v246\n"
    "v_mfma_f32_16x16x32_bf16 v[148:151], a[136:139], a[168:171], v[148:151]\n"
    "v_and_b32 v244, s73, v244\n"
    "v_and_b32 v246, s73, v246\n"
    "v_mul_f32 v245, v245, v10\n"
    "v_mfma_f32_16x16x32_bf16 v[152:155], a[136:139], a[176:179], v[152:155]\n"
    "v_mul_f32 v247, v247, v74\n"
    "v_mul_f32 v244, v244, v42\n"
    "v_mfma_f32_16x16x32_bf16 v[152:155], a[140:143], a[180:183], v[152:155]\n"
    "v_mul_f32 v246, v246, v237\n"
    "v_cvt_pk_bf16_f32 v236, v245, v244\n"
    "v_cvt_pk_bf16_f32 v237, v247, v246\n"
    "v_mfma_f32_16x16x32_bf16 v[156:159], a[140:143], a[188:191], v[156:159]\n"
    "v_accvgpr_read_b32 v244, a42\n"
    "s_waitcnt lgkmcnt(1)\n"
    "buffer_store_dwordx4 v[252:255], v231, s[44:47], s19 offen offset:0 sc1\n"
    "v_mfma_f32_16x16x32_bf16 v[156:159], a[136:139], a[184:187], v[156:159]\n"
    "v_accvgpr_read_b32 v246, a43\n"
    "v_accvgpr_read_b32 v239, a235\n"
    "v_lshlrev_b32 v245, 16, v244\n"
    "v_mfma_f32_16x16x32_bf16 v[160:163], a[136:139], a[192:195], v[160:163]\n"
    "v_lshlrev_b32 v247, 16, v246\n"
    "v_and_b32 v244, s73, v244\n"
    "v_mfma_f32_16x16x32_bf16 v[160:163], a[140:143], a[196:199], v[160:163]\n"
    "v_and_b32 v246, s73, v246\n"
    "v_mul_f32 v245, v245, v11\n"
    "v_mul_f32 v247, v247, v75\n"
    "v_mfma_f32_16x16x32_bf16 v[164:167], a[140:143], a[204:207], v[164:167]\n"
    "v_mul_f32 v244, v244, v43\n"
    "v_mul_f32 v246, v246, v239\n"
    "v_mfma_f32_16x16x32_bf16 v[164:167], a[136:139], a[200:203], v[164:167]\n"
    "v_cvt_pk_bf16_f32 v238, v245, v244\n"
    "v_cvt_pk_bf16_f32 v239, v247, v246\n"
    "ds_write_b128 v229, v[236:239] offset:4096\n"
    "v_mfma_f32_16x16x32_bf16 v[168:171], a[136:139], a[208:211], v[168:171]\n"
    "ds_read_b128 v[240:243], v232 offset:4096\n"
    "ds_read_b128 v[252:255], v232 offset:5120\n"
    "v_mfma_f32_16x16x32_bf16 v[168:171], a[140:143], a[212:215], v[168:171]\n"
    "v_accvgpr_read_b32 v244, a12\n"
    "v_accvgpr_read_b32 v246, a13\n"
    "v_accvgpr_read_b32 v237, a236\n"
    "v_mfma_f32_16x16x32_bf16 v[172:175], a[140:143], a[220:223], v[172:175]\n"
    "v_lshlrev_b32 v245, 16, v244\n"
    "v_lshlrev_b32 v247, 16, v246\n"
    "v_mfma_f32_16x16x32_bf16 v[172:175], a[136:139], a[216:219], v[172:175]\n"
    "v_and_b32 v244, s73, v244\n"
    "v_and_b32 v246, s73, v246\n"
    "s_waitcnt vmcnt(16)\n"
    "v_mfma_f32_16x16x32_bf16 v[176:179], a[144:147], a[160:163], v[176:179]\n"
    "buffer_load_dwordx4 a[136:139], v224, s[36:39], s60 offen offset:2048 sc1\n"
    "v_mfma_f32_16x16x32_bf16 v[176:179], a[148:151], a[164:167], v[176:179]\n"
    "buffer_load_dwordx4 a[140:143], v224, s[36:39], s60 offen offset:3072 sc1\n"
    "v_mul_f32 v245, v245, v12\n"
    "v_mfma_f32_16x16x32_bf16 v[180:183], a[148:151], a[172:175], v[180:183]\n"
    "s_add_u32 s60, s60, 4352\n"
    "v_mul_f32 v247, v247, v76\n"
    "v_mfma_f32_16x16x32_bf16 v[180:183], a[144:147], a[168:171], v[180:183]\n"
    "v_mul_f32 v244, v244, v44\n"
    "v_mul_f32 v246, v246, v237\n"
    "v_cvt_pk_bf16_f32 v236, v245, v244\n"
    "v_mfma_f32_16x16x32_bf16 v[184:187], a[144:147], a[176:179], v[184:187]\n"
    "v_cvt_pk_bf16_f32 v237, v247, v246\n"
    "v_accvgpr_read_b32 v244, a14\n"
    "v_accvgpr_read_b32 v246, a15\n"
    "v_mfma_f32_16x16x32_bf16 v[184:187], a[148:151], a[180:183], v[184:187]\n"
    "v_accvgpr_read_b32 v239, a237\n"
    "v_lshlrev_b32 v245, 16, v244\n"
    "v_mfma_f32_16x16x32_bf16 v[188:191], a[148:151], a[188:191], v[188:191]\n"
    "v_lshlrev_b32 v247, 16, v246\n"
    "v_and_b32 v244, s73, v244\n"
    "v_and_b32 v246, s73, v246\n"
    "v_mfma_f32_16x16x32_bf16 v[188:191], a[144:147], a[184:187], v[188:191]\n"
    "v_mul_f32 v245, v245, v13\n"
    "v_mul_f32 v247, v247, v77\n"
    "v_mfma_f32_16x16x32_bf16 v[192:195], a[144:147], a[192:195], v[192:195]\n"
    "v_mul_f32 v244, v244, v45\n"
    "s_waitcnt lgkmcnt(1)\n"
    "buffer_store_dwordx4 v[240:243], v231, s[44:47], s20 offen offset:0 sc1\n"
    "v_mfma_f32_16x16x32_bf16 v[192:195], a[148:151], a[196:199], v[192:195]\n"
    "v_mul_f32 v246, v246, v239\n"
    "v_cvt_pk_bf16_f32 v238, v245, v244\n"
    "v_mfma_f32_16x16x32_bf16 v[196:199], a[148:151], a[204:207], v[196:199]\n"
    "v_cvt_pk_bf16_f32 v239, v247, v246\n"
    "ds_write_b128 v228, v[236:239] offset:6144\n"
    "v_mfma_f32_16x16x32_bf16 v[196:199], a[144:147], a[200:203], v[196:199]\n"
    "v_accvgpr_read_b32 v244, a44\n"
    "v_accvgpr_read_b32 v246, a45\n"
    "v_mfma_f32_16x16x32_bf16 v[200:203], a[144:147], a[208:211], v[200:203]\n"
    "v_accvgpr_read_b32 v237, a238\n"
    "v_lshlrev_b32 v245, 16, v244\n"
    "v_lshlrev_b32 v247, 16, v246\n"
    "v_mfma_f32_16x16x32_bf16 v[200:203], a[148:151], a[212:215], v[200:203]\n"
    "v_and_b32 v244, s73, v244\n"
    "v_and_b32 v246, s73, v246\n"
    "v_mfma_f32_16x16x32_bf16 v[204:207], a[148:151], a[220:223], v[204:207]\n"
    "v_mul_f32 v245, v245, v14\n"
    "v_mul_f32 v247, v247, v78\n"
    "v_mul_f32 v244, v244, v46\n"
    "v_mfma_f32_16x16x32_bf16 v[204:207], a[144:147], a[216:219], v[204:207]\n"
    "buffer_load_dwordx4 a[144:147], v224, s[36:39], s60 offen offset:0 sc1\n"
    "s_waitcnt vmcnt(18)\n"
    "v_mfma_f32_16x16x32_bf16 a[240:243], a[152:155], a[160:163], a[240:243]\n"
    "ds_read_b128 a[160:163], v226 offset:32768\n"
    "buffer_load_dwordx4 a[148:151], v224, s[36:39], s60 offen offset:1024 sc1\n"
    "v_mfma_f32_16x16x32_bf16 a[240:243], a[156:159], a[164:167], a[240:243]\n"
    "ds_read_b128 a[164:167], v227 offset:32768\n"
    "v_mul_f32 v246, v246, v237\n"
    "v_mfma_f32_16x16x32_bf16 a[244:247], a[156:159], a[172:175], a[244:247]\n"
    "ds_read_b128 a[172:175], v227 offset:34816\n"
    "v_cvt_pk_bf16_f32 v236, v245, v244\n"
    "v_cvt_pk_bf16_f32 v237, v247, v246\n"
    "v_mfma_f32_16x16x32_bf16 a[244:247], a[152:155], a[168:171], a[244:247]\n"
    "ds_read_b128 a[168:171], v226 offset:34816\n"
    "v_accvgpr_read_b32 v244, a46\n"
    "v_mfma_f32_16x16x32_bf16 a[248:251], a[152:155], a[176:179], a[248:251]\n"
    "ds_read_b128 a[176:179], v226 offset:36864\n"
    "s_waitcnt lgkmcnt(6)\n"
    "buffer_store_dwordx4 v[252:255], v231, s[44:47], s21 offen offset:0 sc1\n"
    "v_mfma_f32_16x16x32_bf16 a[248:251], a[156:159], a[180:183], a[248:251]\n"
    "ds_read_b128 a[180:183], v227 offset:36864\n"
    "v_accvgpr_read_b32 v246, a47\n"
    "v_mfma_f32_16x16x32_bf16 a[252:255], a[156:159], a[188:191], a[252:255]\n"
    "ds_read_b128 a[188:191], v227 offset:38912\n"
    "v_accvgpr_read_b32 v239, a239\n"
    "v_lshlrev_b32 v245, 16, v244\n"
    "v_mfma_f32_16x16x32_bf16 a[252:255], a[152:155], a[184:187], a[252:255]\n"
    "ds_read_b128 a[184:187], v226 offset:38912\n"
    "v_lshlrev_b32 v247, 16, v246\n"
    "v_mfma_f32_16x16x32_bf16 v[208:211], a[152:155], a[192:195], v[208:211]\n"
    "ds_read_b128 a[192:195], v226 offset:40960\n"
    "v_and_b32 v244, s73, v244\n"
    "v_and_b32 v246, s73, v246\n"
    "v_mfma_f32_16x16x32_bf16 v[208:211], a[156:159], a[196:199], v[208:211]\n"
    "ds_read_b128 a[196:199], v227 offset:40960\n"
    "v_mul_f32 v245, v245, v15\n"
    "v_mfma_f32_16x16x32_bf16 v[212:215], a[156:159], a[204:207], v[212:215]\n"
    "ds_read_b128 a[204:207], v227 offset:43008\n"
    "v_mul_f32 v247, v247, v79\n"
    "v_mul_f32 v244, v244, v47\n"
    "v_mfma_f32_16x16x32_bf16 v[212:215], a[152:155], a[200:203], v[212:215]\n"
    "ds_read_b128 a[200:203], v226 offset:43008\n"
    "v_mul_f32 v246, v246, v239\n"
    "v_mfma_f32_16x16x32_bf16 v[216:219], a[152:155], a[208:211], v[216:219]\n"
    "ds_read_b128 a[208:211], v226 offset:45056\n"
    "v_cvt_pk_bf16_f32 v238, v245, v244\n"
    "v_cvt_pk_bf16_f32 v239, v247, v246\n"
    "v_mfma_f32_16x16x32_bf16 v[216:219], a[156:159], a[212:215], v[216:219]\n"
    "ds_read_b128 a[212:215], v227 offset:45056\n"
    "ds_write_b128 v229, v[236:239] offset:6144\n"
    "v_mfma_f32_16x16x32_bf16 v[220:223], a[156:159], a[220:223], v[220:223]\n"
    "ds_read_b128 a[220:223], v227 offset:47104\n"
    "ds_read_b128 v[240:243], v232 offset:6144\n"
    "ds_read_b128 v[252:255], v232 offset:7168\n"
    "v_mfma_f32_16x16x32_bf16 v[220:223], a[152:155], a[216:219], v[220:223]\n"
    "ds_read_b128 a[216:219], v226 offset:47104\n"
    "s_waitcnt vmcnt(18) lgkmcnt(11)\n"
    "v_mfma_f32_16x16x32_bf16 v[112:115], a[96:99], a[160:163], v[112:115]\n"
    "buffer_load_dwordx4 a[152:155], v224, s[36:39], s60 offen offset:2048 sc1\n"
    "ds_read_b128 v[248:251], v235 offset:0\n"
    "v_mfma_f32_16x16x32_bf16 v[112:115], a[100:103], a[164:167], v[112:115]\n"
    "buffer_load_dwordx4 a[156:159], v224, s[36:39], s60 offen offset:3072 sc1\n"
    "v_mfma_f32_16x16x32_bf16 v[116:119], a[100:103], a[172:175], v[116:119]\n"
    "s_add_u32 s60, s60, 4352\n"
    "v_accvgpr_read_b32 v244, a16\n"
    "v_accvgpr_read_b32 v246, a17\n"
    "v_mfma_f32_16x16x32_bf16 v[116:119], a[96:99], a[168:171], v[116:119]\n"
    "v_lshlrev_b32 v245, 16, v244\n"
    "v_lshlrev_b32 v247, 16, v246\n"
    "v_and_b32 v244, s73, v244\n"
    "v_mfma_f32_16x16x32_bf16 v[120:123], a[96:99], a[176:179], v[120:123]\n"
    "v_and_b32 v246, s73, v246\n"
    "v_mul_f32 v245, v245, v16\n"
    "v_mfma_f32_16x16x32_bf16 v[120:123], a[100:103], a[180:183], v[120:123]\n"
    "v_mul_f32 v247, v247, v80\n"
    "v_mul_f32 v244, v244, v48\n"
    "v_mul_f32 v246, v246, v96\n"
    "v_mfma_f32_16x16x32_bf16 v[124:127], a[100:103], a[188:191], v[124:127]\n"
    "v_cvt_pk_bf16_f32 v236, v245, v244\n"
    "v_cvt_pk_bf16_f32 v237, v247, v246\n"
    "v_mfma_f32_16x16x32_bf16 v[124:127], a[96:99], a[184:187], v[124:127]\n"
    "v_accvgpr_read_b32 v244, a18\n"
    "v_accvgpr_read_b32 v246, a19\n"
    "s_waitcnt lgkmcnt(1)\n"
    "v_mfma_f32_16x16x32_bf16 v[128:131], a[96:99], a[192:195], v[128:131]\n"
    "v_lshlrev_b32 v245, 16, v244\n"
    "v_lshlrev_b32 v247, 16, v246\n"
    "v_mfma_f32_16x16x32_bf16 v[128:131], a[100:103], a[196:199], v[128:131]\n"
    "v_and_b32 v244, s73, v244\n"
    "v_and_b32 v246, s73, v246\n"
    "v_mul_f32 v245, v245, v17\n"
    "v_mfma_f32_16x16x32_bf16 v[132:135], a[100:103], a[204:207], v[132:135]\n"
    "v_mul_f32 v247, v247, v81\n"
    "v_mul_f32 v244, v244, v49\n"
    "v_mfma_f32_16x16x32_bf16 v[132:135], a[96:99], a[200:203], v[132:135]\n"
    "v_mul_f32 v246, v246, v97\n"
    "buffer_store_dwordx4 v[240:243], v231, s[44:47], s22 offen offset:0 sc1\n"
    "v_mfma_f32_16x16x32_bf16 v[136:139], a[96:99], a[208:211], v[136:139]\n"
    "v_cvt_pk_bf16_f32 v238, v245, v244\n"
    "v_cvt_pk_bf16_f32 v239, v247, v246\n"
    "v_mfma_f32_16x16x32_bf16 v[136:139], a[100:103], a[212:215], v[136:139]\n"
    "ds_write_b128 v228, v[236:239] offset:8192\n"
    "v_mfma_f32_16x16x32_bf16 v[140:143], a[100:103], a[220:223], v[140:143]\n"
    "s_waitcnt lgkmcnt(1)\n"
    "v_lshlrev_b32 v245, 16, v248\n"
    "v_lshlrev_b32 v247, 16, v249\n"
    "v_mfma_f32_16x16x32_bf16 v[140:143], a[96:99], a[216:219], v[140:143]\n"
    "v_and_b32 v244, s73, v248\n"
    "v_and_b32 v246, s73, v249\n"
    "s_waitcnt vmcnt(19)\n"
    "v_mfma_f32_16x16x32_bf16 v[144:147], a[104:107], a[160:163], v[144:147]\n"
    "buffer_load_dwordx4 a[96:99], v224, s[36:39], s60 offen offset:0 sc1\n"
    "v_mfma_f32_16x16x32_bf16 v[144:147], a[108:111], a[164:167], v[144:147]\n"
    "buffer_load_dwordx4 a[100:103], v224, s[36:39], s60 offen offset:1024 sc1\n"
    "v_mul_f32 v245, v245, v18\n"
    "v_mfma_f32_16x16x32_bf16 v[148:151], a[108:111], a[172:175], v[148:151]\n"
    "v_mul_f32 v247, v247, v82\n"
    "v_mul_f32 v244, v244, v50\n"
    "v_mfma_f32_16x16x32_bf16 v[148:151], a[104:107], a[168:171], v[148:151]\n"
    "v_mul_f32 v246, v246, v98\n"
    "v_cvt_pk_bf16_f32 v236, v245, v244\n"
    "v_cvt_pk_bf16_f32 v237, v247, v246\n"
    "v_mfma_f32_16x16x32_bf16 v[152:155], a[104:107], a[176:179], v[152:155]\n"
    "v_lshlrev_b32 v245, 16, v250\n"
    "v_lshlrev_b32 v247, 16, v251\n"
    "v_mfma_f32_16x16x32_bf16 v[152:155], a[108:111], a[180:183], v[152:155]\n"
    "v_and_b32 v244, s73, v250\n"
    "v_and_b32 v246, s73, v251\n"
    "buffer_store_dwordx4 v[252:255], v231, s[44:47], s23 offen offset:0 sc1\n"
    "v_mfma_f32_16x16x32_bf16 v[156:159], a[108:111], a[188:191], v[156:159]\n"
    "v_mul_f32 v245, v245, v19\n"
    "v_mul_f32 v247, v247, v83\n"
    "v_mfma_f32_16x16x32_bf16 v[156:159], a[104:107], a[184:187], v[156:159]\n"
    "v_mul_f32 v244, v244, v51\n"
    "v_mul_f32 v246, v246, v99\n"
    "v_cvt_pk_bf16_f32 v238, v245, v244\n"
    "v_mfma_f32_16x16x32_bf16 v[160:163], a[104:107], a[192:195], v[160:163]\n"
    "v_cvt_pk_bf16_f32 v239, v247, v246\n"
    "ds_write_b128 v229, v[236:239] offset:8192\n"
    "v_mfma_f32_16x16x32_bf16 v[160:163], a[108:111], a[196:199], v[160:163]\n"
    "ds_read_b128 v[240:243], v232 offset:8192\n"
    "ds_read_b128 v[252:255], v232 offset:9216\n"
    "ds_read_b128 v[248:251], v235 offset:1024\n"
    "v_mfma_f32_16x16x32_bf16 v[164:167], a[108:111], a[204:207], v[164:167]\n"
    "v_accvgpr_read_b32 v244, a20\n"
    "v_accvgpr_read_b32 v246, a21\n"
    "v_mfma_f32_16x16x32_bf16 v[164:167], a[104:107], a[200:203], v[164:167]\n"
    "v_lshlrev_b32 v245, 16, v244\n"
    "v_lshlrev_b32 v247, 16, v246\n"
    "v_and_b32 v244, s73, v244\n"
    "v_mfma_f32_16x16x32_bf16 v[168:171], a[104:107], a[208:211], v[168:171]\n"
    "v_and_b32 v246, s73, v246\n"
    "v_mul_f32 v245, v245, v20\n"
    "v_mfma_f32_16x16x32_bf16 v[168:171], a[108:111], a[212:215], v[168:171]\n"
    "v_mul_f32 v247, v247, v84\n"
    "v_mul_f32 v244, v244, v52\n"
    "v_mul_f32 v246, v246, v100\n"
    "v_mfma_f32_16x16x32_bf16 v[172:175], a[108:111], a[220:223], v[172:175]\n"
    "v_cvt_pk_bf16_f32 v236, v245, v244\n"
    "v_cvt_pk_bf16_f32 v237, v247, v246\n"
    "v_mfma_f32_16x16x32_bf16 v[172:175], a[104:107], a[216:219], v[172:175]\n"
    "v_accvgpr_read_b32 v244, a22\n"
    "v_accvgpr_read_b32 v246, a23\n"
    "s_waitcnt vmcnt(19)\n"
    "v_mfma_f32_16x16x32_bf16 v[176:179], a[112:115], a[160:163], v[176:179]\n"
    "buffer_load_dwordx4 a[104:107], v224, s[36:39], s60 offen offset:2048 sc1\n"
    "v_mfma_f32_16x16x32_bf16 v[176:179], a[116:119], a[164:167], v[176:179]\n"
    "buffer_load_dwordx4 a[108:111], v224, s[36:39], s60 offen offset:3072 sc1\n"
    "v_lshlrev_b32 v245, 16, v244\n"
    "v_mfma_f32_16x16x32_bf16 v[180:183], a[116:119], a[172:175], v[180:183]\n"
    "s_add_u32 s60, s60, 4352\n"
    "v_lshlrev_b32 v247, 16, v246\n"
    "v_mfma_f32_16x16x32_bf16 v[180:183], a[112:115], a[168:171], v[180:183]\n"
    "v_and_b32 v244, s73, v244\n"
    "v_and_b32 v246, s73, v246\n"
    "v_mul_f32 v245, v245, v21\n"
    "v_mfma_f32_16x16x32_bf16 v[184:187], a[112:115], a[176:179], v[184:187]\n"
    "v_mul_f32 v247, v247, v85\n"
    "v_mul_f32 v244, v244, v53\n"
    "v_mul_f32 v246, v246, v101\n"
    "v_mfma_f32_16x16x32_bf16 v[184:187], a[116:119], a[180:183], v[184:187]\n"
    "s_waitcnt lgkmcnt(2)\n"
    "buffer_store_dwordx4 v[240:243], v231, s[44:47], s24 offen offset:0 sc1\n"
    "v_mfma_f32_16x16x32_bf16 v[188:191], a[116:119], a[188:191], v[188:191]\n"
    "v_cvt_pk_bf16_f32 v238, v245, v244\n"
    "v_cvt_pk_bf16_f32 v239, v247, v246\n"
    "ds_write_b128 v228, v[236:239] offset:10240\n"
    "v_mfma_f32_16x16x32_bf16 v[188:191], a[112:115], a[184:187], v[188:191]\n"
    "s_waitcnt lgkmcnt(1)\n"
    "v_lshlrev_b32 v245, 16, v248\n"
    "v_lshlrev_b32 v247, 16, v249\n"
    "v_mfma_f32_16x16x32_bf16 v[192:195], a[112:115], a[192:195], v[192:195]\n"
    "v_and_b32 v244, s73, v248\n"
    "v_and_b32 v246, s73, v249\n"
    "v_mul_f32 v245, v245, v22\n"
    "v_mfma_f32_16x16x32_bf16 v[192:195], a[116:119], a[196:199], v[192:195]\n"
    "v_mul_f32 v247, v247, v86\n"
    "v_mul_f32 v244, v244, v54\n"
    "v_mfma_f32_16x16x32_bf16 v[196:199], a[116:119], a[204:207], v[196:199]\n"
    "v_mul_f32 v246, v246, v102\n"
    "v_cvt_pk_bf16_f32 v236, v245, v244\n"
    "v_cvt_pk_bf16_f32 v237, v247, v246\n"
    "v_mfma_f32_16x16x32_bf16 v[196:199], a[112:115], a[200:203], v[196:199]\n"
    "v_lshlrev_b32 v245, 16, v250\n"
    "v_lshlrev_b32 v247, 16, v251\n"
    "v_mfma_f32_16x16x32_bf16 v[200:203], a[112:115], a[208:211], v[200:203]\n"
    "v_and_b32 v244, s73, v250\n"
    "v_and_b32 v246, s73, v251\n"
    "buffer_store_dwordx4 v[252:255], v231, s[44:47], s25 offen offset:0 sc1\n"
    "v_mfma_f32_16x16x32_bf16 v[200:203], a[116:119], a[212:215], v[200:203]\n"
    "v_mul_f32 v245, v245, v23\n"
    "v_mul_f32 v247, v247, v87\n"
    "v_mfma_f32_16x16x32_bf16 v[204:207], a[116:119], a[220:223], v[204:207]\n"
    "v_mul_f32 v244, v244, v55\n"
    "v_mul_f32 v246, v246, v103\n"
    "v_cvt_pk_bf16_f32 v238, v245, v244\n"
    "v_mfma_f32_16x16x32_bf16 v[204:207], a[112:115], a[216:219], v[204:207]\n"
    "buffer_load_dwordx4 a[112:115], v224, s[36:39], s60 offen offset:0 sc1\n"
    "s_waitcnt vmcnt(21)\n"
    "v_mfma_f32_16x16x32_bf16 a[240:243], a[120:123], a[160:163], a[240:243]\n"
    "ds_read_b128 a[160:163], v226 offset:49152\n"
    "buffer_load_dwordx4 a[116:119], v224, s[36:39], s60 offen offset:1024 sc1\n"
    "v_mfma_f32_16x16x32_bf16 a[240:243], a[124:127], a[164:167], a[240:243]\n"
    "ds_read_b128 a[164:167], v227 offset:49152\n"
    "v_cvt_pk_bf16_f32 v239, v247, v246\n"
    "v_mfma_f32_16x16x32_bf16 a[244:247], a[124:127], a[172:175], a[244:247]\n"
    "ds_read_b128 a[172:175], v227 offset:51200\n"
    "ds_write_b128 v229, v[236:239] offset:10240\n"
    "v_mfma_f32_16x16x32_bf16 a[244:247], a[120:123], a[168:171], a[244:247]\n"
    "ds_read_b128 a[168:171], v226 offset:51200\n"
    "ds_read_b128 v[240:243], v232 offset:10240\n"
    "v_mfma_f32_16x16x32_bf16 a[248:251], a[120:123], a[176:179], a[248:251]\n"
    "ds_read_b128 a[176:179], v226 offset:53248\n"
    "ds_read_b128 v[252:255], v232 offset:11264\n"
    "ds_read_b128 v[248:251], v235 offset:2048\n"
    "v_mfma_f32_16x16x32_bf16 a[248:251], a[124:127], a[180:183], a[248:251]\n"
    "ds_read_b128 a[180:183], v227 offset:53248\n"
    "v_accvgpr_read_b32 v244, a24\n"
    "v_mfma_f32_16x16x32_bf16 a[252:255], a[124:127], a[188:191], a[252:255]\n"
    "ds_read_b128 a[188:191], v227 offset:55296\n"
    "v_accvgpr_read_b32 v246, a25\n"
    "v_lshlrev_b32 v245, 16, v244\n"
    "v_mfma_f32_16x16x32_bf16 a[252:255], a[120:123], a[184:187], a[252:255]\n"
    "ds_read_b128 a[184:187], v226 offset:55296\n"
    "v_lshlrev_b32 v247, 16, v246\n"
    "v_mfma_f32_16x16x32_bf16 v[208:211], a[120:123], a[192:195], v[208:211]\n"
    "ds_read_b128 a[192:195], v226 offset:57344\n"
    "v_and_b32 v244, s73, v244\n"
    "v_and_b32 v246, s73, v246\n"
    "v_mfma_f32_16x16x32_bf16 v[208:211], a[124:127], a[196:199], v[208:211]\n"
    "ds_read_b128 a[196:199], v227 offset:57344\n"
    "v_mul_f32 v245, v245, v24\n"
    "v_mfma_f32_16x16x32_bf16 v[212:215], a[124:127], a[204:207], v[212:215]\n"
    "ds_read_b128 a[204:207], v227 offset:59392\n"
    "v_mul_f32 v247, v247, v88\n"
    "v_mul_f32 v244, v244, v56\n"
    "v_mfma_f32_16x16x32_bf16 v[212:215], a[120:123], a[200:203], v[212:215]\n"
    "ds_read_b128 a[200:203], v226 offset:59392\n"
    "v_mul_f32 v246, v246, v104\n"
    "v_mfma_f32_16x16x32_bf16 v[216:219], a[120:123], a[208:211], v[216:219]\n"
    "ds_read_b128 a[208:211], v226 offset:61440\n"
    "v_cvt_pk_bf16_f32 v236, v245, v244\n"
    "v_cvt_pk_bf16_f32 v237, v247, v246\n"
    "v_mfma_f32_16x16x32_bf16 v[216:219], a[124:127], a[212:215], v[216:219]\n"
    "ds_read_b128 a[212:215], v227 offset:61440\n"
    "v_accvgpr_read_b32 v244, a26\n"
    "v_mfma_f32_16x16x32_bf16 v[220:223], a[124:127], a[220:223], v[220:223]\n"
    "ds_read_b128 a[220:223], v227 offset:63488\n"
    "v_accvgpr_read_b32 v246, a27\n"
    "v_lshlrev_b32 v245, 16, v244\n"
    "v_mfma_f32_16x16x32_bf16 v[220:223], a[120:123], a[216:219], v[220:223]\n"
    "ds_read_b128 a[216:219], v226 offset:63488\n"
    "s_waitcnt vmcnt(19) lgkmcnt(8)\n"
    "v_mfma_f32_16x16x32_bf16 v[112:115], a[128:131], a[160:163], v[112:115]\n"
    "buffer_load_dwordx4 a[120:123], v224, s[36:39], s60 offen offset:2048 sc1\n"
    "v_lshlrev_b32 v247, 16, v246\n"
    "v_mfma_f32_16x16x32_bf16 v[112:115], a[132:135], a[164:167], v[112:115]\n"
    "buffer_load_dwordx4 a[124:127], v224, s[36:39], s60 offen offset:3072 sc1\n"
    "v_mfma_f32_16x16x32_bf16 v[116:119], a[132:135], a[172:175], v[116:119]\n"
    "s_add_u32 s60, s60, 4352\n"
    "v_and_b32 v244, s73, v244\n"
    "v_and_b32 v246, s73, v246\n"
    "v_mfma_f32_16x16x32_bf16 v[116:119], a[128:131], a[168:171], v[116:119]\n"
    "v_mul_f32 v245, v245, v25\n"
    "v_mul_f32 v247, v247, v89\n"
    "v_mul_f32 v244, v244, v57\n"
    "v_mfma_f32_16x16x32_bf16 v[120:123], a[128:131], a[176:179], v[120:123]\n"
    "v_mul_f32 v246, v246, v105\n"
    "buffer_store_dwordx4 v[240:243], v231, s[44:47], s26 offen offset:0 sc1\n"
    "v_mfma_f32_16x16x32_bf16 v[120:123], a[132:135], a[180:183], v[120:123]\n"
    "v_cvt_pk_bf16_f32 v238, v245, v244\n"
    "v_cvt_pk_bf16_f32 v239, v247, v246\n"
    "ds_write_b128 v228, v[236:239] offset:12288\n"
    "v_mfma_f32_16x16x32_bf16 v[124:127], a[132:135], a[188:191], v[124:127]\n"
    "v_lshlrev_b32 v245, 16, v248\n"
    "v_lshlrev_b32 v247, 16, v249\n"
    "v_mfma_f32_16x16x32_bf16 v[124:127], a[128:131], a[184:187], v[124:127]\n"
    "v_and_b32 v244, s73, v248\n"
    "v_and_b32 v246, s73, v249\n"
    "s_waitcnt lgkmcnt(1)\n"
    "v_mfma_f32_16x16x32_bf16 v[128:131], a[128:131], a[192:195], v[128:131]\n"
    "v_mul_f32 v245, v245, v26\n"
    "v_mul_f32 v247, v247, v90\n"
    "v_mfma_f32_16x16x32_bf16 v[128:131], a[132:135], a[196:199], v[128:131]\n"
    "v_mul_f32 v244, v244, v58\n"
    "v_mul_f32 v246, v246, v106\n"
    "v_cvt_pk_bf16_f32 v236, v245, v244\n"
    "v_mfma_f32_16x16x32_bf16 v[132:135], a[132:135], a[204:207], v[132:135]\n"
    "v_cvt_pk_bf16_f32 v237, v247, v246\n"
    "v_lshlrev_b32 v245, 16, v250\n"
    "v_mfma_f32_16x16x32_bf16 v[132:135], a[128:131], a[200:203], v[132:135]\n"
    "v_lshlrev_b32 v247, 16, v251\n"
    "v_and_b32 v244, s73, v250\n"
    "v_and_b32 v246, s73, v251\n"
    "v_mfma_f32_16x16x32_bf16 v[136:139], a[128:131], a[208:211], v[136:139]\n"
    "buffer_store_dwordx4 v[252:255], v231, s[44:47], s27 offen offset:0 sc1\n"
    "v_mfma_f32_16x16x32_bf16 v[136:139], a[132:135], a[212:215], v[136:139]\n"
    "v_mul_f32 v245, v245, v27\n"
    "v_mul_f32 v247, v247, v91\n"
    "v_mul_f32 v244, v244, v59\n"
    "v_mfma_f32_16x16x32_bf16 v[140:143], a[132:135], a[220:223], v[140:143]\n"
    "v_mul_f32 v246, v246, v107\n"
    "v_cvt_pk_bf16_f32 v238, v245, v244\n"
    "v_mfma_f32_16x16x32_bf16 v[140:143], a[128:131], a[216:219], v[140:143]\n"
    "v_cvt_pk_bf16_f32 v239, v247, v246\n"
    "ds_write_b128 v229, v[236:239] offset:12288\n"
    "s_waitcnt vmcnt(20)\n"
    "v_mfma_f32_16x16x32_bf16 v[144:147], a[136:139], a[160:163], v[144:147]\n"
    "buffer_load_dwordx4 a[128:131], v224, s[36:39], s60 offen offset:0 sc1\n"
    "v_mfma_f32_16x16x32_bf16 v[144:147], a[140:143], a[164:167], v[144:147]\n"
    "buffer_load_dwordx4 a[132:135], v224, s[36:39], s60 offen offset:1024 sc1\n"
    "ds_read_b128 v[240:243], v232 offset:12288\n"
    "v_mfma_f32_16x16x32_bf16 v[148:151], a[140:143], a[172:175], v[148:151]\n"
    "ds_read_b128 v[252:255], v232 offset:13312\n"
    "ds_read_b128 v[248:251], v235 offset:3072\n"
    "v_mfma_f32_16x16x32_bf16 v[148:151], a[136:139], a[168:171], v[148:151]\n"
    "v_accvgpr_read_b32 v244, a28\n"
    "v_accvgpr_read_b32 v246, a29\n"
    "v_lshlrev_b32 v245, 16, v244\n"
    "v_mfma_f32_16x16x32_bf16 v[152:155], a[136:139], a[176:179], v[152:155]\n"
    "v_lshlrev_b32 v247, 16, v246\n"
    "v_and_b32 v244, s73, v244\n"
    "v_mfma_f32_16x16x32_bf16 v[152:155], a[140:143], a[180:183], v[152:155]\n"
    "v_and_b32 v246, s73, v246\n"
    "v_mul_f32 v245, v245, v28\n"
    "v_mul_f32 v247, v247, v92\n"
    "v_mfma_f32_16x16x32_bf16 v[156:159], a[140:143], a[188:191], v[156:159]\n"
    "v_mul_f32 v244, v244, v60\n"
    "v_mul_f32 v246, v246, v108\n"
    "v_mfma_f32_16x16x32_bf16 v[156:159], a[136:139], a[184:187], v[156:159]\n"
    "v_cvt_pk_bf16_f32 v236, v245, v244\n"
    "v_cvt_pk_bf16_f32 v237, v247, v246\n"
    "v_accvgpr_read_b32 v244, a30\n"
    "v_mfma_f32_16x16x32_bf16 v[160:163], a[136:139], a[192:195], v[160:163]\n"
    "v_accvgpr_read_b32 v246, a31\n"
    "v_lshlrev_b32 v245, 16, v244\n"
    "v_mfma_f32_16x16x32_bf16 v[160:163], a[140:143], a[196:199], v[160:163]\n"
    "v_lshlrev_b32 v247, 16, v246\n"
    "v_and_b32 v244, s73, v244\n"
    "v_and_b32 v246, s73, v246\n"
    "v_mfma_f32_16x16x32_bf16 v[164:167], a[140:143], a[204:207], v[164:167]\n"
    "v_mul_f32 v245, v245, v29\n"
    "v_mul_f32 v247, v247, v93\n"
    "v_mfma_f32_16x16x32_bf16 v[164:167], a[136:139], a[200:203], v[164:167]\n"
    "v_mul_f32 v244, v244, v61\n"
    "v_mul_f32 v246, v246, v109\n"
    "s_waitcnt lgkmcnt(2)\n"
    "buffer_store_dwordx4 v[240:243], v231, s[44:47], s28 offen offset:0 sc1\n"
    "v_mfma_f32_16x16x32_bf16 v[168:171], a[136:139], a[208:211], v[168:171]\n"
    "v_cvt_pk_bf16_f32 v238, v245, v244\n"
    "v_cvt_pk_bf16_f32 v239, v247, v246\n"
    "v_mfma_f32_16x16x32_bf16 v[168:171], a[140:143], a[212:215], v[168:171]\n"
    "ds_write_b128 v228, v[236:239] offset:14336\n"
    "v_mfma_f32_16x16x32_bf16 v[172:175], a[140:143], a[220:223], v[172:175]\n"
    "s_waitcnt lgkmcnt(1)\n"
    "v_lshlrev_b32 v245, 16, v248\n"
    "v_lshlrev_b32 v247, 16, v249\n"
    "v_mfma_f32_16x16x32_bf16 v[172:175], a[136:139], a[216:219], v[172:175]\n"
    "v_and_b32 v244, s73, v248\n"
    "v_and_b32 v246, s73, v249\n"
    "s_waitcnt vmcnt(20)\n"
    "v_mfma_f32_16x16x32_bf16 v[176:179], a[144:147], a[160:163], v[176:179]\n"
    "buffer_load_dwordx4 a[136:139], v224, s[36:39], s60 offen offset:2048 sc1\n"
    "v_mfma_f32_16x16x32_bf16 v[176:179], a[148:151], a[164:167], v[176:179]\n"
    "buffer_load_dwordx4 a[140:143], v224, s[36:39], s60 offen offset:3072 sc1\n"
    "v_mul_f32 v245, v245, v30\n"
    "v_mfma_f32_16x16x32_bf16 v[180:183], a[148:151], a[172:175], v[180:183]\n"
    "s_add_u32 s60, s60, 4352\n"
    "v_mul_f32 v247, v247, v94\n"
    "v_mfma_f32_16x16x32_bf16 v[180:183], a[144:147], a[168:171], v[180:183]\n"
    "v_mul_f32 v244, v244, v62\n"
    "v_mul_f32 v246, v246, v110\n"
    "v_cvt_pk_bf16_f32 v236, v245, v244\n"
    "v_mfma_f32_16x16x32_bf16 v[184:187], a[144:147], a[176:179], v[184:187]\n"
    "v_cvt_pk_bf16_f32 v237, v247, v246\n"
    "v_lshlrev_b32 v245, 16, v250\n"
    "v_lshlrev_b32 v247, 16, v251\n"
    "v_mfma_f32_16x16x32_bf16 v[184:187], a[148:151], a[180:183], v[184:187]\n"
    "v_and_b32 v244, s73, v250\n"
    "v_and_b32 v246, s73, v251\n"
    "v_mfma_f32_16x16x32_bf16 v[188:191], a[148:151], a[188:191], v[188:191]\n"
    "buffer_store_dwordx4 v[252:255], v231, s[44:47], s29 offen offset:0 sc1\n"
    "v_mfma_f32_16x16x32_bf16 v[188:191], a[144:147], a[184:187], v[188:191]\n"
    "v_mul_f32 v245, v245, v31\n"
    "v_mul_f32 v247, v247, v95\n"
    "v_mfma_f32_16x16x32_bf16 v[192:195], a[144:147], a[192:195], v[192:195]\n"
    "v_mul_f32 v244, v244, v63\n"
    "v_mul_f32 v246, v246, v111\n"
    "v_cvt_pk_bf16_f32 v238, v245, v244\n"
    "v_mfma_f32_16x16x32_bf16 v[192:195], a[148:151], a[196:199], v[192:195]\n"
    "v_cvt_pk_bf16_f32 v239, v247, v246\n"
    "ds_write_b128 v229, v[236:239] offset:14336\n"
    "v_mfma_f32_16x16x32_bf16 v[196:199], a[148:151], a[204:207], v[196:199]\n"
    "ds_read_b128 v[240:243], v232 offset:14336\n"
    "ds_read_b128 v[252:255], v232 offset:15360\n"
    "v_mfma_f32_16x16x32_bf16 v[196:199], a[144:147], a[200:203], v[196:199]\n"
    "v_mfma_f32_16x16x32_bf16 v[200:203], a[144:147], a[208:211], v[200:203]\n"
    "v_mfma_f32_16x16x32_bf16 v[200:203], a[148:151], a[212:215], v[200:203]\n"
    "v_add_u32 v226, 0, v225\n"
    "v_xor_b32 v227, 64, v226\n"
    "v_mfma_f32_16x16x32_bf16 v[204:207], a[148:151], a[220:223], v[204:207]\n"
    "s_waitcnt lgkmcnt(2)\n"
    "s_barrier\n"
    "v_mfma_f32_16x16x32_bf16 v[204:207], a[144:147], a[216:219], v[204:207]\n"
    "buffer_load_dwordx4 a[144:147], v224, s[36:39], s60 offen offset:0 sc1\n"
    "s_waitcnt vmcnt(21)\n"
    "v_mfma_f32_16x16x32_bf16 a[240:243], a[152:155], a[160:163], a[240:243]\n"
    "ds_read_b128 a[160:163], v226 offset:0\n"
    "buffer_load_dwordx4 a[148:151], v224, s[36:39], s60 offen offset:1024 sc1\n"
    "v_mfma_f32_16x16x32_bf16 a[240:243], a[156:159], a[164:167], a[240:243]\n"
    "ds_read_b128 a[164:167], v227 offset:0\n"
    "v_mfma_f32_16x16x32_bf16 a[244:247], a[156:159], a[172:175], a[244:247]\n"
    "ds_read_b128 a[172:175], v227 offset:2048\n"
    "s_add_u32 s61, s61, 1\n"
    "v_mfma_f32_16x16x32_bf16 a[244:247], a[152:155], a[168:171], a[244:247]\n"
    "ds_read_b128 a[168:171], v226 offset:2048\n"
    "v_mfma_f32_16x16x32_bf16 a[248:251], a[152:155], a[176:179], a[248:251]\n"
    "ds_read_b128 a[176:179], v226 offset:4096\n"
    "s_add_u32 s74, s74, 2048\n"
    "v_mfma_f32_16x16x32_bf16 a[248:251], a[156:159], a[180:183], a[248:251]\n"
    "ds_read_b128 a[180:183], v227 offset:4096\n"
    "v_mfma_f32_16x16x32_bf16 a[252:255], a[156:159], a[188:191], a[252:255]\n"
    "ds_read_b128 a[188:191], v227 offset:6144\n"
    "v_mfma_f32_16x16x32_bf16 a[252:255], a[152:155], a[184:187], a[252:255]\n"
    "ds_read_b128 a[184:187], v226 offset:6144\n"
    "v_mfma_f32_16x16x32_bf16 v[208:211], a[152:155], a[192:195], v[208:211]\n"
    "ds_read_b128 a[192:195], v226 offset:8192\n"
    "v_mfma_f32_16x16x32_bf16 v[208:211], a[156:159], a[196:199], v[208:211]\n"
    "ds_read_b128 a[196:199], v227 offset:8192\n"
    "v_mfma_f32_16x16x32_bf16 v[212:215], a[156:159], a[204:207], v[212:215]\n"
    "ds_read_b128 a[204:207], v227 offset:10240\n"
    "s_waitcnt lgkmcnt(12)\n"
    "buffer_store_dwordx4 v[240:243], v231, s[44:47], s30 offen offset:0 sc1\n"
    "v_mfma_f32_16x16x32_bf16 v[212:215], a[152:155], a[200:203], v[212:215]\n"
    "ds_read_b128 a[200:203], v226 offset:10240\n"
    "v_mfma_f32_16x16x32_bf16 v[216:219], a[152:155], a[208:211], v[216:219]\n"
    "ds_read_b128 a[208:211], v226 offset:12288\n"
    "v_mfma_f32_16x16x32_bf16 v[216:219], a[156:159], a[212:215], v[216:219]\n"
    "ds_read_b128 a[212:215], v227 offset:12288\n"
    "v_mfma_f32_16x16x32_bf16 v[220:223], a[156:159], a[220:223], v[220:223]\n"
    "ds_read_b128 a[220:223], v227 offset:14336\n"
    "v_mfma_f32_16x16x32_bf16 v[220:223], a[152:155], a[216:219], v[220:223]\n"
    "ds_read_b128 a[216:219], v226 offset:14336\n"
    "s_waitcnt lgkmcnt(15)\n"
    "buffer_store_dwordx4 v[252:255], v231, s[44:47], s31 offen offset:0 sc1\n"
    "s_cmp_lg_u32 s61, 3\n"
    "s_cbranch_scc1 L_S1_%=\n"
    "v_add_u32 v228, s71, v230\n"
    "v_xor_b32 v229, 16, v228\n"
    "v_add_u32 v232, s71, v224\n"
    "s_nop 3\n"
    "v_accvgpr_read_b32 v244, a48\n"
    "v_accvgpr_read_b32 v246, a49\n"
    "v_accvgpr_read_b32 v237, a240\n"
    "v_lshlrev_b32 v245, 16, v244\n"
    "v_lshlrev_b32 v247, 16, v246\n"
    "v_and_b32 v244, s73, v244\n"
    "v_and_b32 v246, s73, v246\n"
    "v_mul_f32 v245, v245, v112\n"
    "v_mul_f32 v247, v247, v176\n"
    "v_mul_f32 v244, v244, v144\n"
    "v_mul_f32 v246, v246, v237\n"
    "v_cvt_pk_bf16_f32 v236, v245, v244\n"
    "v_cvt_pk_bf16_f32 v237, v247, v246\n"
    "v_accvgpr_read_b32 v244, a50\n"
    "v_accvgpr_read_b32 v246, a51\n"
    "v_accvgpr_read_b32 v239, a241\n"
    "v_lshlrev_b32 v245, 16, v244\n"
    "v_lshlrev_b32 v247, 16, v246\n"
    "v_and_b32 v244, s73, v244\n"
    "v_and_b32 v246, s73, v246\n"
    "v_mul_f32 v245, v245, v113\n"
    "v_mul_f32 v247, v247, v177\n"
    "v_mul_f32 v244, v244, v145\n"
    "v_mul_f32 v246, v246, v239\n"
    "v_cvt_pk_bf16_f32 v238, v245, v244\n"
    "v_cvt_pk_bf16_f32 v239, v247, v246\n"
    "ds_write_b128 v228, v[236:239] offset:0\n"
    "v_accvgpr_read_b32 v244, a80\n"
    "v_accvgpr_read_b32 v246, a81\n"
    "v_accvgpr_read_b32 v237, a242\n"
    "v_lshlrev_b32 v245, 16, v244\n"
    "v_lshlrev_b32 v247, 16, v246\n"
    "v_and_b32 v244, s73, v244\n"
    "v_and_b32 v246, s73, v246\n"
    "v_mul_f32 v245, v245, v114\n"
    "v_mul_f32 v247, v247, v178\n"
    "v_mul_f32 v244, v244, v146\n"
    "v_mul_f32 v246, v246, v237\n"
    "v_cvt_pk_bf16_f32 v236, v245, v244\n"
    "v_cvt_pk_bf16_f32 v237, v247, v246\n"
    "v_accvgpr_read_b32 v244, a82\n"
    "v_accvgpr_read_b32 v246, a83\n"
    "v_accvgpr_read_b32 v239, a243\n"
    "v_lshlrev_b32 v245, 16, v244\n"
    "v_lshlrev_b32 v247, 16, v246\n"
    "v_and_b32 v244, s73, v244\n"
    "v_and_b32 v246, s73, v246\n"
    "v_mul_f32 v245, v245, v115\n"
    "v_mul_f32 v247, v247, v179\n"
    "v_mul_f32 v244, v244, v147\n"
    "v_mul_f32 v246, v246, v239\n"
    "v_cvt_pk_bf16_f32 v238, v245, v244\n"
    "v_cvt_pk_bf16_f32 v239, v247, v246\n"
    "ds_write_b128 v229, v[236:239] offset:0\n"
    "ds_read_b128 v[240:243], v232 offset:0\n"
    "ds_read_b128 v[252:255], v232 offset:1024\n"
    "v_accvgpr_read_b32 v244, a52\n"
    "v_accvgpr_read_b32 v246, a53\n"
    "v_accvgpr_read_b32 v237, a244\n"
    "v_lshlrev_b32 v245, 16, v244\n"
    "v_lshlrev_b32 v247, 16, v246\n"
    "v_and_b32 v244, s73, v244\n"
    "v_and_b32 v246, s73, v246\n"
    "v_mul_f32 v245, v245, v116\n"
    "v_mul_f32 v247, v247, v180\n"
    "v_mul_f32 v244, v244, v148\n"
    "v_mul_f32 v246, v246, v237\n"
    "v_cvt_pk_bf16_f32 v236, v245, v244\n"
    "v_cvt_pk_bf16_f32 v237, v247, v246\n"
    "v_accvgpr_read_b32 v244, a54\n"
    "v_accvgpr_read_b32 v246, a55\n"
    "v_accvgpr_read_b32 v239, a245\n"
    "v_lshlrev_b32 v245, 16, v244\n"
    "v_lshlrev_b32 v247, 16, v246\n"
    "v_and_b32 v244, s73, v244\n"
    "v_and_b32 v246, s73, v246\n"
    "v_mul_f32 v245, v245, v117\n"
    "v_mul_f32 v247, v247, v181\n"
    "v_mul_f32 v244, v244, v149\n"
    "s_waitcnt lgkmcnt(1)\n"
    "buffer_store_dwordx4 v[240:243], v231, s[44:47], s77 offen offset:128 sc1\n"
    "v_mul_f32 v246, v246, v239\n"
    "v_cvt_pk_bf16_f32 v238, v245, v244\n"
    "v_cvt_pk_bf16_f32 v239, v247, v246\n"
    "ds_write_b128 v228, v[236:239] offset:2048\n"
    "v_accvgpr_read_b32 v244, a84\n"
    "v_accvgpr_read_b32 v246, a85\n"
    "v_accvgpr_read_b32 v237, a246\n"
    "v_lshlrev_b32 v245, 16, v244\n"
    "v_lshlrev_b32 v247, 16, v246\n"
    "v_and_b32 v244, s73, v244\n"
    "v_and_b32 v246, s73, v246\n"
    "v_mul_f32 v245, v245, v118\n"
    "v_mul_f32 v247, v247, v182\n"
    "v_mul_f32 v244, v244, v150\n"
    "v_mul_f32 v246, v246, v237\n"
    "v_cvt_pk_bf16_f32 v236, v245, v244\n"
    "v_cvt_pk_bf16_f32 v237, v247, v246\n"
    "v_accvgpr_read_b32 v244, a86\n"
    "s_waitcnt lgkmcnt(1)\n"
    "buffer_store_dwordx4 v[252:255], v231, s[44:47], s17 offen offset:128 sc1\n"
    "v_accvgpr_read_b32 v246, a87\n"
    "v_accvgpr_read_b32 v239, a247\n"
    "v_lshlrev_b32 v245, 16, v244\n"
    "v_lshlrev_b32 v247, 16, v246\n"
    "v_and_b32 v244, s73, v244\n"
    "v_and_b32 v246, s73, v246\n"
    "v_mul_f32 v245, v245, v119\n"
    "v_mul_f32 v247, v247, v183\n"
    "v_mul_f32 v244, v244, v151\n"
    "v_mul_f32 v246, v246, v239\n"
    "v_cvt_pk_bf16_f32 v238, v245, v244\n"
    "v_cvt_pk_bf16_f32 v239, v247, v246\n"
    "ds_write_b128 v229, v[236:239] offset:2048\n"
    "ds_read_b128 v[240:243], v232 offset:2048\n"
    "ds_read_b128 v[252:255], v232 offset:3072\n"
    "v_accvgpr_read_b32 v244, a56\n"
    "v_accvgpr_read_b32 v246, a57\n"
    "v_accvgpr_read_b32 v237, a248\n"
    "v_lshlrev_b32 v245, 16, v244\n"
    "v_lshlrev_b32 v247, 16, v246\n"
    "v_and_b32 v244, s73, v244\n"
    "v_and_b32 v246, s73, v246\n"
    "v_mul_f32 v245, v245, v120\n"
    "v_mul_f32 v247, v247, v184\n"
    "v_mul_f32 v244, v244, v152\n"
    "v_mul_f32 v246, v246, v237\n"
    "v_cvt_pk_bf16_f32 v236, v245, v244\n"
    "v_cvt_pk_bf16_f32 v237, v247, v246\n"
    "v_accvgpr_read_b32 v244, a58\n"
    "v_accvgpr_read_b32 v246, a59\n"
    "v_accvgpr_read_b32 v239, a249\n"
    "v_lshlrev_b32 v245, 16, v244\n"
    "v_lshlrev_b32 v247, 16, v246\n"
    "v_and_b32 v244, s73, v244\n"
    "v_and_b32 v246, s73, v246\n"
    "v_mul_f32 v245, v245, v121\n"
    "v_mul_f32 v247, v247, v185\n"
    "v_mul_f32 v244, v244, v153\n"
    "s_waitcnt lgkmcnt(1)\n"
    "buffer_store_dwordx4 v[240:243], v231, s[44:47], s18 offen offset:128 sc1\n"
    "v_mul_f32 v246, v246, v239\n"
    "v_cvt_pk_bf16_f32 v238, v245, v244\n"
    "v_cvt_pk_bf16_f32 v239, v247, v246\n"
    "ds_write_b128 v228, v[236:239] offset:4096\n"
    "v_accvgpr_read_b32 v244, a88\n"
    "v_accvgpr_read_b32 v246, a89\n"
    "v_accvgpr_read_b32 v237, a250\n"
    "v_lshlrev_b32 v245, 16, v244\n"
    "v_lshlrev_b32 v247, 16, v246\n"
    "v_and_b32 v244, s73, v244\n"
    "v_and_b32 v246, s73, v246\n"
    "v_mul_f32 v245, v245, v122\n"
    "v_mul_f32 v247, v247, v186\n"
    "v_mul_f32 v244, v244, v154\n"
    "v_mul_f32 v246, v246, v237\n"
    "v_cvt_pk_bf16_f32 v236, v245, v244\n"
    "v_cvt_pk_bf16_f32 v237, v247, v246\n"
    "v_accvgpr_read_b32 v244, a90\n"
    "s_waitcnt lgkmcnt(1)\n"
    "buffer_store_dwordx4 v[252:255], v231, s[44:47], s19 offen offset:128 sc1\n"
    "v_accvgpr_read_b32 v246, a91\n"
    "v_accvgpr_read_b32 v239, a251\n"
    "v_lshlrev_b32 v245, 16, v244\n"
    "v_lshlrev_b32 v247, 16, v246\n"
    "v_and_b32 v244, s73, v244\n"
    "v_and_b32 v246, s73, v246\n"
    "v_mul_f32 v245, v245, v123\n"
    "v_mul_f32 v247, v247, v187\n"
    "v_mul_f32 v244, v244, v155\n"
    "v_mul_f32 v246, v246, v239\n"
    "v_cvt_pk_bf16_f32 v238, v245, v244\n"
    "v_cvt_pk_bf16_f32 v239, v247, v246\n"
    "ds_write_b128 v229, v[236:239] offset:4096\n"
    "ds_read_b128 v[240:243], v232 offset:4096\n"
    "ds_read_b128 v[252:255], v232 offset:5120\n"
    "v_accvgpr_read_b32 v244, a60\n"
    "v_accvgpr_read_b32 v246, a61\n"
    "v_accvgpr_read_b32 v237, a252\n"
    "v_lshlrev_b32 v245, 16, v244\n"
    "v_lshlrev_b32 v247, 16, v246\n"
    "v_and_b32 v244, s73, v244\n"
    "v_and_b32 v246, s73, v246\n"
    "v_mul_f32 v245, v245, v124\n"
    "v_mul_f32 v247, v247, v188\n"
    "v_mul_f32 v244, v244, v156\n"
    "v_mul_f32 v246, v246, v237\n"
    "v_cvt_pk_bf16_f32 v236, v245, v244\n"
    "v_cvt_pk_bf16_f32 v237, v247, v246\n"
    "v_accvgpr_read_b32 v244, a62\n"
    "v_accvgpr_read_b32 v246, a63\n"
    "v_accvgpr_read_b32 v239, a253\n"
    "v_lshlrev_b32 v245, 16, v244\n"
    "v_lshlrev_b32 v247, 16, v246\n"
    "v_and_b32 v244, s73, v244\n"
    "v_and_b32 v246, s73, v246\n"
    "v_mul_f32 v245, v245, v125\n"
    "v_mul_f32 v247, v247, v189\n"
    "v_mul_f32 v244, v244, v157\n"
    "s_waitcnt lgkmcnt(1)\n"
    "buffer_store_dwordx4 v[240:243], v231, s[44:47], s20 offen offset:128 sc1\n"
    "v_mul_f32 v246, v246, v239\n"
    "v_cvt_pk_bf16_f32 v238, v245, v244\n"
    "v_cvt_pk_bf16_f32 v239, v247, v246\n"
    "ds_write_b128 v228, v[236:239] offset:6144\n"
    "v_accvgpr_read_b32 v244, a92\n"
    "v_accvgpr_read_b32 v246, a93\n"
    "v_accvgpr_read_b32 v237, a254\n"
    "v_lshlrev_b32 v245, 16, v244\n"
    "v_lshlrev_b32 v247, 16, v246\n"
    "v_and_b32 v244, s73, v244\n"
    "v_and_b32 v246, s73, v246\n"
    "v_mul_f32 v245, v245, v126\n"
    "v_mul_f32 v247, v247, v190\n"
    "v_mul_f32 v244, v244, v158\n"
    "v_mul_f32 v246, v246, v237\n"
    "v_cvt_pk_bf16_f32 v236, v245, v244\n"
    "v_cvt_pk_bf16_f32 v237, v247, v246\n"
    "v_accvgpr_read_b32 v244, a94\n"
    "s_waitcnt lgkmcnt(1)\n"
    "buffer_store_dwordx4 v[252:255], v231, s[44:47], s21 offen offset:128 sc1\n"
    "v_accvgpr_read_b32 v246, a95\n"
    "v_accvgpr_read_b32 v239, a255\n"
    "v_lshlrev_b32 v245, 16, v244\n"
    "v_lshlrev_b32 v247, 16, v246\n"
    "v_and_b32 v244, s73, v244\n"
    "v_and_b32 v246, s73, v246\n"
    "v_mul_f32 v245, v245, v127\n"
    "v_mul_f32 v247, v247, v191\n"
    "v_mul_f32 v244, v244, v159\n"
    "v_mul_f32 v246, v246, v239\n"
    "v_cvt_pk_bf16_f32 v238, v245, v244\n"
    "v_cvt_pk_bf16_f32 v239, v247, v246\n"
    "ds_write_b128 v229, v[236:239] offset:6144\n"
    "ds_read_b128 v[240:243], v232 offset:6144\n"
    "ds_read_b128 v[252:255], v232 offset:7168\n"
    "ds_read_b128 v[248:251], v235 offset:4096\n"
    "v_accvgpr_read_b32 v244, a64\n"
    "v_accvgpr_read_b32 v246, a65\n"
    "v_lshlrev_b32 v245, 16, v244\n"
    "v_lshlrev_b32 v247, 16, v246\n"
    "v_and_b32 v244, s73, v244\n"
    "v_and_b32 v246, s73, v246\n"
    "v_mul_f32 v245, v245, v128\n"
    "v_mul_f32 v247, v247, v192\n"
    "v_mul_f32 v244, v244, v160\n"
    "v_mul_f32 v246, v246, v208\n"
    "v_cvt_pk_bf16_f32 v236, v245, v244\n"
    "v_cvt_pk_bf16_f32 v237, v247, v246\n"
    "v_accvgpr_read_b32 v244, a66\n"
    "v_accvgpr_read_b32 v246, a67\n"
    "v_lshlrev_b32 v245, 16, v244\n"
    "v_lshlrev_b32 v247, 16, v246\n"
    "v_and_b32 v244, s73, v244\n"
    "v_and_b32 v246, s73, v246\n"
    "v_mul_f32 v245, v245, v129\n"
    "v_mul_f32 v247, v247, v193\n"
    "v_mul_f32 v244, v244, v161\n"
    "v_mul_f32 v246, v246, v209\n"
    "s_waitcnt lgkmcnt(2)\n"
    "buffer_store_dwordx4 v[240:243], v231, s[44:47], s22 offen offset:128 sc1\n"
    "v_cvt_pk_bf16_f32 v238, v245, v244\n"
    "v_cvt_pk_bf16_f32 v239, v247, v246\n"
    "ds_write_b128 v228, v[236:239] offset:8192\n"
    "s_waitcnt lgkmcnt(1)\n"
    "v_lshlrev_b32 v245, 16, v248\n"
    "v_lshlrev_b32 v247, 16, v249\n"
    "v_and_b32 v244, s73, v248\n"
    "v_and_b32 v246, s73, v249\n"
    "v_mul_f32 v245, v245, v130\n"
    "v_mul_f32 v247, v247, v194\n"
    "v_mul_f32 v244, v244, v162\n"
    "v_mul_f32 v246, v246, v210\n"
    "v_cvt_pk_bf16_f32 v236, v245, v244\n"
    "v_cvt_pk_bf16_f32 v237, v247, v246\n"
    "v_lshlrev_b32 v245, 16, v250\n"
    "v_lshlrev_b32 v247, 16, v251\n"
    "v_and_b32 v244, s73, v250\n"
    "v_and_b32 v246, s73, v251\n"
    "buffer_store_dwordx4 v[252:255], v231, s[44:47], s23 offen offset:128 sc1\n"
    "v_mul_f32 v245, v245, v131\n"
    "v_mul_f32 v247, v247, v195\n"
    "v_mul_f32 v244, v244, v163\n"
    "v_mul_f32 v246, v246, v211\n"
    "v_cvt_pk_bf16_f32 v238, v245, v244\n"
    "v_cvt_pk_bf16_f32 v239, v247, v246\n"
    "ds_write_b128 v229, v[236:239] offset:8192\n"
    "ds_read_b128 v[240:243], v232 offset:8192\n"
    "ds_read_b128 v[252:255], v232 offset:9216\n"
    "ds_read_b128 v[248:251], v235 offset:5120\n"
    "v_accvgpr_read_b32 v244, a68\n"
    "v_accvgpr_read_b32 v246, a69\n"
    "v_lshlrev_b32 v245, 16, v244\n"
    "v_lshlrev_b32 v247, 16, v246\n"
    "v_and_b32 v244, s73, v244\n"
    "v_and_b32 v246, s73, v246\n"
    "v_mul_f32 v245, v245, v132\n"
    "v_mul_f32 v247, v247, v196\n"
    "v_mul_f32 v244, v244, v164\n"
    "v_mul_f32 v246, v246, v212\n"
    "v_cvt_pk_bf16_f32 v236, v245, v244\n"
    "v_cvt_pk_bf16_f32 v237, v247, v246\n"
    "v_accvgpr_read_b32 v244, a70\n"
    "v_accvgpr_read_b32 v246, a71\n"
    "v_lshlrev_b32 v245, 16, v244\n"
    "v_lshlrev_b32 v247, 16, v246\n"
    "v_and_b32 v244, s73, v244\n"
    "v_and_b32 v246, s73, v246\n"
    "v_mul_f32 v245, v245, v133\n"
    "v_mul_f32 v247, v247, v197\n"
    "v_mul_f32 v244, v244, v165\n"
    "v_mul_f32 v246, v246, v213\n"
    "s_waitcnt lgkmcnt(2)\n"
    "buffer_store_dwordx4 v[240:243], v231, s[44:47], s24 offen offset:128 sc1\n"
    "v_cvt_pk_bf16_f32 v238, v245, v244\n"
    "v_cvt_pk_bf16_f32 v239, v247, v246\n"
    "ds_write_b128 v228, v[236:239] offset:10240\n"
    "s_waitcnt lgkmcnt(1)\n"
    "v_lshlrev_b32 v245, 16, v248\n"
    "v_lshlrev_b32 v247, 16, v249\n"
    "v_and_b32 v244, s73, v248\n"
    "v_and_b32 v246, s73, v249\n"
    "v_mul_f32 v245, v245, v134\n"
    "v_mul_f32 v247, v247, v198\n"
    "v_mul_f32 v244, v244, v166\n"
    "v_mul_f32 v246, v246, v214\n"
    "v_cvt_pk_bf16_f32 v236, v245, v244\n"
    "v_cvt_pk_bf16_f32 v237, v247, v246\n"
    "v_lshlrev_b32 v245, 16, v250\n"
    "v_lshlrev_b32 v247, 16, v251\n"
    "v_and_b32 v244, s73, v250\n"
    "v_and_b32 v246, s73, v251\n"
    "buffer_store_dwordx4 v[252:255], v231, s[44:47], s25 offen offset:128 sc1\n"
    "v_mul_f32 v245, v245, v135\n"
    "v_mul_f32 v247, v247, v199\n"
    "v_mul_f32 v244, v244, v167\n"
    "v_mul_f32 v246, v246, v215\n"
    "v_cvt_pk_bf16_f32 v238, v245, v244\n"
    "v_cvt_pk_bf16_f32 v239, v247, v246\n"
    "ds_write_b128 v229, v[236:239] offset:10240\n"
    "ds_read_b128 v[240:243], v232 offset:10240\n"
    "ds_read_b128 v[252:255], v232 offset:11264\n"
    "ds_read_b128 v[248:251], v235 offset:6144\n"
    "v_accvgpr_read_b32 v244, a72\n"
    "v_accvgpr_read_b32 v246, a73\n"
    "v_lshlrev_b32 v245, 16, v244\n"
    "v_lshlrev_b32 v247, 16, v246\n"
    "v_and_b32 v244, s73, v244\n"
    "v_and_b32 v246, s73, v246\n"
    "v_mul_f32 v245, v245, v136\n"
    "v_mul_f32 v247, v247, v200\n"
    "v_mul_f32 v244, v244, v168\n"
    "v_mul_f32 v246, v246, v216\n"
    "v_cvt_pk_bf16_f32 v236, v245, v244\n"
    "v_cvt_pk_bf16_f32 v237, v247, v246\n"
    "v_accvgpr_read_b32 v244, a74\n"
    "v_accvgpr_read_b32 v246, a75\n"
    "v_lshlrev_b32 v245, 16, v244\n"
    "v_lshlrev_b32 v247, 16, v246\n"
    "v_and_b32 v244, s73, v244\n"
    "v_and_b32 v246, s73, v246\n"
    "v_mul_f32 v245, v245, v137\n"
    "v_mul_f32 v247, v247, v201\n"
    "v_mul_f32 v244, v244, v169\n"
    "v_mul_f32 v246, v246, v217\n"
    "s_waitcnt lgkmcnt(2)\n"
    "buffer_store_dwordx4 v[240:243], v231, s[44:47], s26 offen offset:128 sc1\n"
    "v_cvt_pk_bf16_f32 v238, v245, v244\n"
    "v_cvt_pk_bf16_f32 v239, v247, v246\n"
    "ds_write_b128 v228, v[236:239] offset:12288\n"
    "s_waitcnt lgkmcnt(1)\n"
    "v_lshlrev_b32 v245, 16, v248\n"
    "v_lshlrev_b32 v247, 16, v249\n"
    "v_and_b32 v244, s73, v248\n"
    "v_and_b32 v246, s73, v249\n"
    "v_mul_f32 v245, v245, v138\n"
    "v_mul_f32 v247, v247, v202\n"
    "v_mul_f32 v244, v244, v170\n"
    "v_mul_f32 v246, v246, v218\n"
    "v_cvt_pk_bf16_f32 v236, v245, v244\n"
    "v_cvt_pk_bf16_f32 v237, v247, v246\n"
    "v_lshlrev_b32 v245, 16, v250\n"
    "v_lshlrev_b32 v247, 16, v251\n"
    "v_and_b32 v244, s73, v250\n"
    "v_and_b32 v246, s73, v251\n"
    "buffer_store_dwordx4 v[252:255], v231, s[44:47], s27 offen offset:128 sc1\n"
    "v_mul_f32 v245, v245, v139\n"
    "v_mul_f32 v247, v247, v203\n"
    "v_mul_f32 v244, v244, v171\n"
    "v_mul_f32 v246, v246, v219\n"
    "v_cvt_pk_bf16_f32 v238, v245, v244\n"
    "v_cvt_pk_bf16_f32 v239, v247, v246\n"
    "ds_write_b128 v229, v[236:239] offset:12288\n"
    "ds_read_b128 v[240:243], v232 offset:12288\n"
    "ds_read_b128 v[252:255], v232 offset:13312\n"
    "ds_read_b128 v[248:251], v235 offset:7168\n"
    "v_accvgpr_read_b32 v244, a76\n"
    "v_accvgpr_read_b32 v246, a77\n"
    "v_lshlrev_b32 v245, 16, v244\n"
    "v_lshlrev_b32 v247, 16, v246\n"
    "v_and_b32 v244, s73, v244\n"
    "v_and_b32 v246, s73, v246\n"
    "v_mul_f32 v245, v245, v140\n"
    "v_mul_f32 v247, v247, v204\n"
    "v_mul_f32 v244, v244, v172\n"
    "v_mul_f32 v246, v246, v220\n"
    "v_cvt_pk_bf16_f32 v236, v245, v244\n"
    "v_cvt_pk_bf16_f32 v237, v247, v246\n"
    "v_accvgpr_read_b32 v244, a78\n"
    "v_accvgpr_read_b32 v246, a79\n"
    "v_lshlrev_b32 v245, 16, v244\n"
    "v_lshlrev_b32 v247, 16, v246\n"
    "v_and_b32 v244, s73, v244\n"
    "v_and_b32 v246, s73, v246\n"
    "v_mul_f32 v245, v245, v141\n"
    "v_mul_f32 v247, v247, v205\n"
    "v_mul_f32 v244, v244, v173\n"
    "v_mul_f32 v246, v246, v221\n"
    "s_waitcnt lgkmcnt(2)\n"
    "buffer_store_dwordx4 v[240:243], v231, s[44:47], s28 offen offset:128 sc1\n"
    "v_cvt_pk_bf16_f32 v238, v245, v244\n"
    "v_cvt_pk_bf16_f32 v239, v247, v246\n"
    "ds_write_b128 v228, v[236:239] offset:14336\n"
    "s_waitcnt lgkmcnt(1)\n"
    "v_lshlrev_b32 v245, 16, v248\n"
    "v_lshlrev_b32 v247, 16, v249\n"
    "v_and_b32 v244, s73, v248\n"
    "v_and_b32 v246, s73, v249\n"
    "v_mul_f32 v245, v245, v142\n"
    "v_mul_f32 v247, v247, v206\n"
    "v_mul_f32 v244, v244, v174\n"
    "v_mul_f32 v246, v246, v222\n"
    "v_cvt_pk_bf16_f32 v236, v245, v244\n"
    "v_cvt_pk_bf16_f32 v237, v247, v246\n"
    "v_lshlrev_b32 v245, 16, v250\n"
    "v_lshlrev_b32 v247, 16, v251\n"
    "v_and_b32 v244, s73, v250\n"
    "v_and_b32 v246, s73, v251\n"
    "buffer_store_dwordx4 v[252:255], v231, s[44:47], s29 offen offset:128 sc1\n"
    "v_mul_f32 v245, v245, v143\n"
    "v_mul_f32 v247, v247, v207\n"
    "v_mul_f32 v244, v244, v175\n"
    "v_mul_f32 v246, v246, v223\n"
    "v_cvt_pk_bf16_f32 v238, v245, v244\n"
    "v_cvt_pk_bf16_f32 v239, v247, v246\n"
    "ds_write_b128 v229, v[236:239] offset:14336\n"
    "ds_read_b128 v[240:243], v232 offset:14336\n"
    "ds_read_b128 v[252:255], v232 offset:15360\n"
    "s_waitcnt lgkmcnt(1)\n"
    "buffer_store_dwordx4 v[240:243], v231, s[44:47], s30 offen offset:128 sc1\n"
    "s_waitcnt lgkmcnt(0)\n"
    "buffer_store_dwordx4 v[252:255], v231, s[44:47], s31 offen offset:128 sc1\n"
    "s_waitcnt vmcnt(0) lgkmcnt(0)\n"
    "s_branch L_END_%=\n"
    "L_SLOW_%=:\n"
    "s_mov_b32 s75, 0x40000\n"
    "L_SPIN_%=:\n"
    "s_sleep 1\n"
    "buffer_load_dword v233, v224, s[52:55], 0 offen sc1\n"
    "s_waitcnt vmcnt(0)\n"
    "v_cmp_le_u32 vcc, s78, v233\n"
    "s_and_b32 s81, vcc_lo, 15\n"
    "s_cmp_eq_u32 s81, 15\n"
    "s_cbranch_scc1 L_BACK_%=\n"
    "s_sub_u32 s75, s75, 1\n"
    "s_cmp_lg_u32 s75, 0\n"
    "s_cbranch_scc1 L_SPIN_%=\n"
    "s_branch L_BACK_%=\n"
    "L_END_%=:\n"
    "dcn_code_end:\n"
    :: "s"(LO(x0)), "s"(HI(x0)), "s"(LO(W)), "s"(HI(W)), "s"(LO(bs)), "s"(HI(bs)), "s"(LO(ws)), "s"(HI(ws)), "s"(LO(out)), "s"(HI(out)),
       "s"(b), "s"(w), "s"(ldsb), "s"(fb0), "s"(fb1)
    : "v0","v1","v2","v3","v4","v5","v6","v7","v8","v9","v10","v11","v12","v13","v14","v15","v16","v17","v18","v19","v20","v21","v22","v23","v24","v25","v26","v27","v28","v29","v30","v31","v32","v33","v34","v35","v36","v37","v38","v39","v40","v41","v42","v43","v44","v45","v46","v47","v48","v49","v50","v51","v52","v53","v54","v55","v56","v57","v58","v59","v60","v61","v62","v63","v64","v65","v66","v67","v68","v69","v70","v71","v72","v73","v74","v75","v76","v77","v78","v79","v80","v81","v82","v83","v84","v85","v86","v87","v88","v89","v90","v91","v92","v93","v94","v95","v96","v97","v98","v99","v100","v101","v102","v103","v104","v105","v106","v107","v108","v109","v110","v111","v112","v113","v114","v115","v116","v117","v118","v119","v120","v121","v122","v123","v124","v125","v126","v127","v128","v129","v130","v131","v132","v133","v134","v135","v136","v137","v138","v139","v140","v141","v142","v143","v144","v145","v146","v147","v148","v149","v150","v151","v152","v153","v154","v155","v156","v157","v158","v159","v160","v161","v162","v163","v164","v165","v166","v167","v168","v169","v170","v171","v172","v173","v174","v175","v176","v177","v178","v179","v180","v181","v182","v183","v184","v185","v186","v187","v188","v189","v190","v191","v192","v193","v194","v195","v196","v197","v198","v199","v200","v201","v202","v203","v204","v205","v206","v207","v208","v209","v210","v211","v212","v213","v214","v215","v216","v217","v218","v219","v220","v221","v222","v223","v224","v225","v226","v227","v228","v229","v230","v231","v232","v233","v234","v235","v236","v237","v238","v239","v240","v241","v242","v243","v244","v245","v246","v247","v248","v249","v250","v251","v252","v253","v254","v255","a0","a1","a2","a3","a4","a5","a6","a7","a8","a9","a10","a11","a12","a13","a14","a15","a16","a17","a18","a19","a20","a21","a22","a23","a24","a25","a26","a27","a28","a29","a30","a31","a32","a33","a34","a35","a36","a37","a38","a39","a40","a41","a42","a43","a44","a45","a46","a47","a48","a49","a50","a51","a52","a53","a54","a55","a56","a57","a58","a59","a60","a61","a62","a63","a64","a65","a66","a67","a68","a69","a70","a71","a72","a73","a74","a75","a76","a77","a78","a79","a80","a81","a82","a83","a84","a85","a86","a87","a88","a89","a90","a91","a92","a93","a94","a95","a96","a97","a98","a99","a100","a101","a102","a103","a104","a105","a106","a107","a108","a109","a110","a111","a112","a113","a114","a115","a116","a117","a118","a119","a120","a121","a122","a123","a124","a125","a126","a127","a128","a129","a130","a131","a132","a133","a134","a135","a136","a137","a138","a139","a140","a141","a142","a143","a144","a145","a146","a147","a148","a149","a150","a151","a152","a153","a154","a155","a156","a157","a158","a159","a160","a161","a162","a163","a164","a165","a166","a167","a168","a169","a170","a171","a172","a173","a174","a175","a176","a177","a178","a179","a180","a181","a182","a183","a184","a185","a186","a187","a188","a189","a190","a191","a192","a193","a194","a195","a196","a197","a198","a199","a200","a201","a202","a203","a204","a205","a206","a207","a208","a209","a210","a211","a212","a213","a214","a215","a216","a217","a218","a219","a220","a221","a222","a223","a224","a225","a226","a227","a228","a229","a230","a231","a232","a233","a234","a235","a236","a237","a238","a239","a240","a241","a242","a243","a244","a245","a246","a247","a248","a249","a250","a251","a252","a253","a254","a255","s16","s17","s18","s19","s20","s21","s22","s23","s24","s25","s26","s27","s28","s29","s30","s31","s33","s34","s35","s36","s37","s38","s39","s40","s41","s42","s43","s44","s45","s46","s47","s48","s49","s50","s51","s52","s53","s54","s55","s56","s57","s58","s59","s60","s61","s62","s63","s64","s65","s66","s67","s68","s69","s70","s71","s72","s73","s74","s75","s76","s77","s78","s79","s80","s81","s82","s83","s84","s85","s86","s87","s88","s89","s90","s91","s92","s93","s94","s95","s96","s97","s98","s99","vcc","scc","memory");
}

extern "C" void kernel_launch(void* const* d_in, const int* in_sizes, int n_in, void* d_out, int out_size, void* d_ws, size_t ws_size,
                              hipStream_t stream) {
  dcn_main<<<256, 256, 0, stream>>>((const char*)d_in[0], (const char*)d_in[1], (const char*)d_in[2], (char*)d_ws, (char*)d_out);
}
```
